```python
import torch

def _softmax(x, axis=-1):
    return torch.softmax(x, dim=axis)
def _gelu(x):
    return torch.nn.functional.gelu(x, approximate="tanh")
def _silu(x):
    return torch.nn.functional.silu(x)
def _sigmoid(x):
    return torch.sigmoid(x)
def _layernorm(x, axis=-1, eps=1e-5):
    m = x.mean(dim=axis, keepdim=True)
    v = x.var(dim=axis, keepdim=True, unbiased=False)
    return (x - m) / torch.sqrt(v + eps)
def _scatter_add(tgt, idx, src, axis=0):
    return tgt.clone().index_add_(axis, idx, src)
def _segment_sum(src, seg, n):
    out = torch.zeros((n,) + src.shape[1:], dtype=src.dtype)
    return out.index_add_(0, seg, src)
def _pad(x, pw):
    flat = tuple(v for pair in reversed(pw) for v in pair)
    return torch.nn.functional.pad(x, flat)

kernel_name = 'recurrent_scan__ssm_conv_s4__mixer_s_mlp__prompt'

def setup_inputs():

    _w = (lambda a: (lambda t: t.to(torch.bfloat16) if t.is_floating_point() else t)(torch.as_tensor(a)))
    def _A_setup():
        B, L, D, N = 1, 2048, 512, 128
        A_re = -(lambda _x: torch.exp(torch.as_tensor(_x)))((lambda *s: torch.randn(*s, dtype=torch.bfloat16) * 3.2)(D, N))
        A_im = torch.pi * (lambda *s: torch.randn(*s, dtype=torch.bfloat16) * 0.05)(D, N)
        B_re = (lambda *s: torch.randn(*s, dtype=torch.bfloat16) * 0.1)(D, N)
        B_im = (lambda *s: torch.randn(*s, dtype=torch.bfloat16) * 0.1)(D, N)
        C_re = (lambda *s: torch.randn(*s, dtype=torch.bfloat16) * 0.1)(D, N)
        C_im = (lambda *s: torch.randn(*s, dtype=torch.bfloat16) * 0.1)(D, N)
        log_dt = (lambda *s: torch.randn(*s, dtype=torch.bfloat16) * 0.05)(D) * 0.5 - 4.0
        Dskip = (lambda *s: torch.randn(*s, dtype=torch.bfloat16) * 0.05)(D)
        return {
            "u": _w((lambda *s: torch.randn(*s, dtype=torch.bfloat16) * 0.05)(B, L, D)),
            "A_re": _w(A_re),
            "A_im": _w(A_im),
            "B_re": _w(B_re),
            "B_im": _w(B_im),
            "C_re": _w(C_re),
            "C_im": _w(C_im),
            "log_dt": _w(log_dt),
            "Dskip": _w(Dskip),
        }
    _A_in = _A_setup()

    def _B_setup():
        B, S, D, F = 1, 2048, 512, 1376
        return {
            "x":  _w((lambda *s: torch.randn(*s, dtype=torch.bfloat16) * 0.05)(B, S, D)),
            "w1": _w((lambda *s: torch.randn(*s, dtype=torch.bfloat16) * 3.2)(D, D) * (D ** -0.5)),
            "w2": _w((lambda *s: torch.randn(*s, dtype=torch.bfloat16) * 3.2)(D, D) * (D ** -0.5)),
            "w3": _w((lambda *s: torch.randn(*s, dtype=torch.bfloat16) * 3.2)(D, F) * (D ** -0.5)),
            "w4": _w((lambda *s: torch.randn(*s, dtype=torch.bfloat16) * 1.6)(F, D) * (F ** -0.5)),
        }
    _B_in = _B_setup()

    return {
        "A_u": _A_in["u"],
        "A_A_re": _A_in["A_re"],
        "A_A_im": _A_in["A_im"],
        "A_B_re": _A_in["B_re"],
        "A_B_im": _A_in["B_im"],
        "A_C_re": _A_in["C_re"],
        "A_C_im": _A_in["C_im"],
        "A_log_dt": _A_in["log_dt"],
        "A_Dskip": _A_in["Dskip"],
        "B_x": _B_in["x"],
        "B_w1": _B_in["w1"],
        "B_w2": _B_in["w2"],
        "B_w3": _B_in["w3"],
        "B_w4": _B_in["w4"],
    }

def reference(A_u, A_A_re, A_A_im, A_B_re, A_B_im, A_C_re, A_C_im, A_log_dt, A_Dskip, B_x, B_w1, B_w2, B_w3, B_w4):
    def _A_ref(u, A_re, A_im, B_re, B_im, C_re, C_im, log_dt, Dskip):
        Bsz, L, D = u.shape
        dt = torch.exp(log_dt)[:, None]
        A = A_re + 1j * A_im
        Bc = B_re + 1j * B_im
        Cc = C_re + 1j * C_im
        logdA = dt * A
        dB = dt * Bc
        l = (lambda x, dt: x.to(dt))(torch.arange(L), A_re.dtype)
        P = torch.exp(logdA[:, :, None] * l[None, None, :])
        K = torch.einsum("dn,dnl,dn->dl", Cc, P, dB).real
        L2 = 2 * L
        Kp = _pad(K, ((0, 0), (0, L)))
        up = (lambda x, *d: x.permute(*d))(u, 0, 2, 1)
        up = _pad(up, ((0, 0), (0, 0), (0, L)))
        Kf = (lambda x, axes=(-2,-1): torch.fft.rfft2(x.float(), dim=axes))(Kp[:, None, :], axes=(-2, -1))
        Uf = (lambda x, axes=(-2,-1): torch.fft.rfft2(x.float(), dim=axes))(up[:, :, None, :], axes=(-2, -1))
        Yf = Uf * Kf[None, :, :, :]
        y = (lambda x, s, axes=(-2,-1): torch.fft.irfft2(x, s=s, dim=axes).to(torch.bfloat16))(Yf, (1, L2), axes=(-2, -1))[:, :, 0, :L]
        y = (lambda x, *d: x.permute(*d))(y, 0, 2, 1) + u * Dskip[None, None, :]
        return y

    def _B_ref(x, w1, w2, w3, w4):
        B, S, D = x.shape
        h = _gelu(_layernorm(x, axis=-1) @ w1)
        c = D // 4
        g0 = _pad(h[:, :, 0*c:1*c], ((0, 0), (1, 0), (0, 0)))[:, :S, :]
        g1 = _pad(h[:, :, 1*c:2*c], ((0, 0), (0, 1), (0, 0)))[:, 1:, :]
        g2 = _pad(h[:, :, 2*c:3*c], ((0, 0), (2, 0), (0, 0)))[:, :S, :]
        g3 = _pad(h[:, :, 3*c:   ], ((0, 0), (0, 2), (0, 0)))[:, 2:, :]
        hs = torch.concatenate([g0, g1, g2, g3], axis=-1) @ w2
        x1 = x + hs
        h2 = _gelu(_layernorm(x1, axis=-1) @ w3) @ w4
        return x1 + h2

    x = A_u
    xn = _layernorm(x, axis=-1)
    s = _A_ref(xn, A_A_re, A_A_im, A_B_re, A_B_im, A_C_re, A_C_im, A_log_dt, A_Dskip)
    g = _silu(_B_ref(xn, B_w1, B_w2, B_w3, B_w4))
    return x + s * g
```

```cpp
#include <hip/hip_runtime.h>
#include <hip/hip_bf16.h>
#include <stdint.h>

typedef unsigned short u16;
__device__ __forceinline__ float bf2f(u16 v){ return __uint_as_float(((uint32_t)v)<<16); }
__device__ __forceinline__ u16 f2bf(float f){ uint32_t u=__float_as_uint(f); u += 0x7FFF + ((u>>16)&1); return (u16)(u>>16); }
__device__ __forceinline__ float rbf(float f){ return bf2f(f2bf(f)); }

constexpr int L=2048, D=512, N=128, F=1376;

__device__ void ln_row(const float* v8, float* o8){
  float s=0; for(int i=0;i<8;i++) s+=v8[i];
  for(int o=32;o>0;o>>=1) s+=__shfl_xor(s,o);
  float mean=s/512.f;
  float q=0; for(int i=0;i<8;i++){float d=v8[i]-mean; q+=d*d;}
  for(int o=32;o>0;o>>=1) q+=__shfl_xor(q,o);
  float var=rbf(q/512.f);
  float m=rbf(mean);
  float sd=rbf(sqrtf(rbf(var+1e-5f)));
  for(int i=0;i<8;i++) o8[i]=rbf(rbf(v8[i]-m)/sd);
}
__global__ void k_ln2(const u16* x,u16* xn,u16* xn2){
  int t=blockIdx.x, l=threadIdx.x; float a[8],b[8],c[8];
  for(int i=0;i<8;i++) a[i]=bf2f(x[t*D+l*8+i]);
  ln_row(a,b); ln_row(b,c);
  for(int i=0;i<8;i++){ xn[t*D+l*8+i]=f2bf(b[i]); xn2[t*D+l*8+i]=f2bf(c[i]); }
}
__global__ void k_ln1(const u16* x,u16* xn){
  int t=blockIdx.x, l=threadIdx.x; float a[8],b[8];
  for(int i=0;i<8;i++) a[i]=bf2f(x[t*D+l*8+i]);
  ln_row(a,b);
  for(int i=0;i<8;i++){ xn[t*D+l*8+i]=f2bf(b[i]); }
}
__global__ void k_kgen(const u16* Are,const u16* Aim,const u16* Bre,const u16* Bim,const u16* Cre,const u16* Cim,const u16* logdt,float* K){
  int d=blockIdx.y; int l=blockIdx.x*256+threadIdx.x;
  float dt=rbf(expf(bf2f(logdt[d])));
  float lv=rbf((float)l);
  float acc=0;
  for(int n=0;n<N;n++){
    float a=dt*bf2f(Are[d*N+n]), b=dt*bf2f(Aim[d*N+n]);
    float br=dt*bf2f(Bre[d*N+n]), bi=dt*bf2f(Bim[d*N+n]);
    float cr=bf2f(Cre[d*N+n]), ci=bf2f(Cim[d*N+n]);
    float wr=cr*br-ci*bi, wi=cr*bi+ci*br;
    float e=expf(a*lv); float sn,cs; sincosf(b*lv,&sn,&cs);
    acc+= e*(wr*cs-wi*sn);
  }
  K[d*L+l]=acc;
}
__global__ void k_conv(const u16* xn,const float* K,const u16* Dskip,u16* s){
  int d=blockIdx.y; int t=blockIdx.x*256+threadIdx.x;
  float acc=0;
  for(int l=0;l<=t;l++) acc+=K[d*L+l]*bf2f(xn[(t-l)*D+d]);
  float y=rbf(acc);
  s[t*D+d]=f2bf(y+rbf(bf2f(xn[t*D+d])*bf2f(Dskip[d])));
}
__device__ float gelu(float x){ float u=0.7978845608028654f*(x+0.044715f*x*x*x); return 0.5f*x*(1.f+tanhf(u)); }
__global__ void k_gemm(const u16* A,const u16* B,u16* C,int Kk,int Nn,int mode,const u16* res,const u16* s,const u16* x){
  int t=blockIdx.y; int n=blockIdx.x*64+threadIdx.x; if(n>=Nn) return;
  float acc=0;
  for(int k=0;k<Kk;k++){
    float a;
    if(mode==1){ int g=k/128; int sh=(g==0)?-1:(g==1)?1:(g==2)?-2:2; int tt=t+sh; a=(tt>=0&&tt<L)?bf2f(A[tt*Kk+k]):0.f; }
    else a=bf2f(A[t*Kk+k]);
    acc+=a*bf2f(B[k*Nn+n]);
  }
  float r=rbf(acc);
  if(mode==0) C[t*Nn+n]=f2bf(gelu(r));
  else if(mode==1) C[t*Nn+n]=f2bf(bf2f(res[t*Nn+n])+r);
  else { float z=rbf(bf2f(res[t*Nn+n])+r); float g=rbf(z/(1.f+expf(-z))); float p=rbf(bf2f(s[t*Nn+n])*g); C[t*Nn+n]=f2bf(bf2f(x[t*Nn+n])+p); }
}

extern "C" void kernel_launch(void* const* d_in, const int* in_sizes, int n_in,
                              void* d_out, int out_size, void* d_ws, size_t ws_size,
                              hipStream_t stream) {
  const u16* x=(const u16*)d_in[0];
  char* ws=(char*)d_ws;
  u16* xn=(u16*)(ws); u16* xn2=(u16*)(ws+(2<<20)); u16* h=(u16*)(ws+(4<<20)); u16* x1=(u16*)(ws+(6<<20));
  u16* x1n=(u16*)(ws+(8<<20)); u16* H=(u16*)(ws+(10<<20)); u16* s=(u16*)(ws+(16<<20)); float* K=(float*)(ws+(18<<20));
  k_ln2<<<L,64,0,stream>>>(x,xn,xn2);
  k_kgen<<<dim3(L/256,D),256,0,stream>>>((const u16*)d_in[1],(const u16*)d_in[2],(const u16*)d_in[3],(const u16*)d_in[4],(const u16*)d_in[5],(const u16*)d_in[6],(const u16*)d_in[7],K);
  k_conv<<<dim3(L/256,D),256,0,stream>>>(xn,K,(const u16*)d_in[8],s);
  k_gemm<<<dim3(D/64,L),64,0,stream>>>(xn2,(const u16*)d_in[10],h,D,D,0,nullptr,nullptr,nullptr);
  k_gemm<<<dim3(D/64,L),64,0,stream>>>(h,(const u16*)d_in[11],x1,D,D,1,xn,nullptr,nullptr);
  k_ln1<<<L,64,0,stream>>>(x1,x1n);
  k_gemm<<<dim3((F+63)/64,L),64,0,stream>>>(x1n,(const u16*)d_in[12],H,D,F,0,nullptr,nullptr,nullptr);
  k_gemm<<<dim3(D/64,L),64,0,stream>>>(H,(const u16*)d_in[13],(u16*)d_out,F,D,2,x1,s,x);
}
```

```cpp
#include <hip/hip_runtime.h>
#include <stdint.h>

struct Srd { const void* p; uint32_t bytes; uint32_t fmt; };
struct Args { Srd s[15]; };

__global__ void __launch_bounds__(512, 2) s4mix_kernel(Args a) {
  asm volatile(
    ".Ls4_start:\n"
    "s_load_dwordx16 s[16:31], %0, 0x0\n"
    "s_load_dwordx16 s[48:63], %0, 0x80\n"
    "s_load_dwordx8 s[64:71], %0, 0xc0\n"
    "s_load_dwordx4 s[72:75], %0, 0xe0\n"
    "s_load_dwordx16 s[32:47], %0, 0x40\n"
    "s_getpc_b64 s[90:91]\n"
    ".Ls4_pc:\n"
    "v_readfirstlane_b32 s79, %2\n"
    "v_and_b32 v12, 63, %2\n"
    "s_mov_b32 s12, %1\n"
    "s_lshr_b32 s79, s79, 6\n"
    "s_and_b32 s80, s12, 0xf8\n"
    "s_add_u32 s80, s80, s79\n"
    "s_lshl_b32 s80, s80, 8\n"
    "v_and_b32 v6, 1, v12\n"
    "v_lshl_add_u32 v6, v6, 7, s80\n"
    "v_min_u32 v6, .Ls4_end-.Ls4_pc-4, v6\n"
    "global_load_dword v7, v6, s[90:91]\n"
    "s_and_b32 s13, s12, 7\n"
    "s_bfe_u32 s101, s12, 0x20003\n"
    "s_lshl2_add_u32 s13, s13, s101\n"
    "s_lshr_b32 s14, s12, 5\n"
    "s_lshl_b32 s77, s14, 7\n"
    "s_and_b32 s15, s79, 3\n"
    "v_and_b32 v13, 15, v12\n"
    "v_lshrrev_b32 v14, 4, v12\n"
    "v_lshlrev_b32 v0, 4, v12\n"
    "s_lshr_b32 s82, s14, 2\n"
    "s_add_u32 s76, s82, 1\n"
    "s_sub_u32 s81, 0, s76\n"
    "s_bitcmp1_b32 s14, 1\n"
    "s_cselect_b32 s76, s76, s81\n"
    "s_cmp_lt_u32 s79, 4\n"
    "s_cbranch_scc1 .Ls4_roleA\n"
    "s_lshl_b32 s82, s14, 5\n"
    "s_add_u32 s82, s82, s13\n"
    "s_lshr_b32 s83, s15, 1\n"
    "s_lshl1_add_u32 s80, s82, s83\n"
    "s_and_b32 s81, s15, 1\n"
    "s_lshl1_add_u32 s84, s80, s81\n"
    "s_lshl_b32 s84, s84, 7\n"
    "s_lshl_b32 s85, s80, 1\n"
    "v_lshlrev_b32 v11, 1, v12\n"
    "v_mov_b32 v16, 0\n"
    "v_mov_b32 v17, 0\n"
    "v_mov_b32 v18, 0\n"
    "v_mov_b32 v19, 0\n"
    "v_mov_b32 v20, 0\n"
    "v_mov_b32 v21, 0\n"
    "v_mov_b32 v22, 0\n"
    "s_sub_u32 s83, 63, s15\n"
    "s_cmp_lt_i32 s76, 0\n"
    "s_cselect_b32 s83, s83, s15\n"
    "s_lshl_b32 s87, s13, 6\n"
    "s_add_u32 s87, s87, s83\n"
    "s_lshl_b32 s87, s87, 10\n"
    "s_waitcnt lgkmcnt(0)\n"
    "buffer_load_short_d16_hi v16, v11, s[48:51], s84 offen\n"
    "buffer_load_short_d16_hi v17, v11, s[52:55], s84 offen\n"
    "buffer_load_short_d16_hi v18, v11, s[56:59], s84 offen\n"
    "buffer_load_short_d16_hi v19, v11, s[60:63], s84 offen\n"
    "buffer_load_short_d16_hi v20, v11, s[64:67], s84 offen\n"
    "buffer_load_short_d16_hi v21, v11, s[68:71], s84 offen\n"
    "buffer_load_short_d16_hi v22, off, s[72:75], s85\n"
    "buffer_load_dwordx4 v[24:27], v0, s[16:19], s87 offen\n"
    "s_lshl_b32 s95, s79, 8\n"
    "s_add_u32 s95, s95, 161088\n"
    "v_lshl_add_u32 v2, v12, 2, s95\n"
    "v_mov_b32 v3, 0\n"
    "ds_write_b32 v2, v3\n"
    "v_and_b32 v1, 3, v12\n"
    "v_mul_u32_u24 v1, 260, v1\n"
    "v_add_u32 v1, 161088, v1\n"
    "s_mov_b32 s100, 0x40000\n"
    "s_waitcnt lgkmcnt(0)\n"
    "s_barrier\n"
    "s_load_dwordx2 s[96:97], s[24:25], 0x0\n"
    "s_branch .Ls4_roleB\n"
    ".Ls4_roleA:\n"
    "s_setprio 1\n"
    "s_lshl_b32 s82, s82, 3\n"
    "s_lshl2_add_u32 s80, s13, s15\n"
    "s_lshl4_add_u32 s83, s80, s76\n"
    "v_or_b32 v92, s82, v14\n"
    "v_xor_b32 v93, v92, v13\n"
    "v_lshlrev_b32 v93, 4, v93\n"
    "s_and_b32 s78, s14, 3\n"
    "s_xor_b32 s78, s78, s101\n"
    "s_lshl_b32 s85, s78, 8\n"
    "s_lshl_b32 s84, s15, 14\n"
    "v_add_u32 v94, s83, v92\n"
    "v_and_b32 v94, 2047, v94\n"
    "v_lshl_or_b32 v64, v94, 10, v93\n"
    "v_xor_b32 v94, 4, v92\n"
    "v_add_u32 v94, s83, v94\n"
    "v_xor_b32 v95, 64, v93\n"
    "v_and_b32 v94, 2047, v94\n"
    "v_lshl_or_b32 v65, v94, 10, v95\n"
    "v_xor_b32 v94, 8, v92\n"
    "v_add_u32 v94, s83, v94\n"
    "v_xor_b32 v95, 128, v93\n"
    "v_and_b32 v94, 2047, v94\n"
    "v_lshl_or_b32 v66, v94, 10, v95\n"
    "v_xor_b32 v94, 12, v92\n"
    "v_add_u32 v94, s83, v94\n"
    "v_xor_b32 v95, 192, v93\n"
    "v_and_b32 v94, 2047, v94\n"
    "v_lshl_or_b32 v67, v94, 10, v95\n"
    "v_lshrrev_b32 v94, 3, v12\n"
    "v_and_b32 v72, 7, v12\n"
    "v_lshlrev_b32 v72, 4, v72\n"
    "v_lshl_add_u32 v72, v94, 10, v72\n"
    "v_and_b32 v94, 1, v14\n"
    "v_lshlrev_b32 v94, 5, v94\n"
    "v_xor_b32 v68, v94, v72\n"
    "v_xor_b32 v71, 64, v68\n"
    "v_add_u32 v69, 7168, v71\n"
    "v_add_u32 v70, 14336, v68\n"
    "v_add_u32 v71, 21504, v71\n"
    "v_add_u32 v73, 7168, v72\n"
    "s_lshl_b32 s86, s15, 15\n"
    "s_add_u32 s86, s86, s77\n"
    "s_lshl_b32 s88, s78, 17\n"
    "s_add_u32 s86, s86, s88\n"
    "s_lshl_b32 s87, s15, 12\n"
    "s_waitcnt lgkmcnt(0)\n"
    "s_add_u32 m0, s84, 0\n"
    "s_nop 0\n"
    "buffer_load_dwordx4 v64, s[16:19], s85 offen lds\n"
    "s_add_u32 m0, s84, 1024\n"
    "s_nop 0\n"
    "buffer_load_dwordx4 v65, s[16:19], s85 offen lds\n"
    "s_add_u32 m0, s84, 2048\n"
    "s_nop 0\n"
    "buffer_load_dwordx4 v66, s[16:19], s85 offen lds\n"
    "s_add_u32 m0, s84, 3072\n"
    "s_nop 0\n"
    "buffer_load_dwordx4 v67, s[16:19], s85 offen lds\n"
    "s_add_u32 m0, s87, 65536\n"
    "s_nop 0\n"
    "buffer_load_dwordx4 v68, s[20:23], s86 offen offset:0 lds\n"
    "buffer_load_dwordx4 v69, s[20:23], s86 offen offset:1024 lds\n"
    "buffer_load_dwordx4 v70, s[20:23], s86 offen offset:2048 lds\n"
    "buffer_load_dwordx4 v71, s[20:23], s86 offen offset:3072 lds\n"
    "s_lshl_b32 s95, s79, 8\n"
    "s_add_u32 s95, s95, 161088\n"
    "v_lshl_add_u32 v2, v12, 2, s95\n"
    "v_mov_b32 v3, 0\n"
    "ds_write_b32 v2, v3\n"
    "v_lshlrev_b32 v11, 2, v12\n"
    "v_add_u32 v11, 160064, v11\n"
    "ds_write_b32 v11, v3\n"
    "ds_write_b32 v11, v3 offset:256\n"
    "v_and_b32 v1, 3, v12\n"
    "v_mul_u32_u24 v1, 260, v1\n"
    "v_add_u32 v1, 161088, v1\n"
    "s_mov_b32 s100, 0x40000\n"
    "s_waitcnt lgkmcnt(0)\n"
    "s_barrier\n"
    "s_load_dwordx2 s[96:97], s[24:25], 0x0\n"
    "s_xor_b32 s88, s85, 256\n"
    "s_add_u32 m0, s84, 4096\n"
    "s_nop 0\n"
    "buffer_load_dwordx4 v64, s[16:19], s88 offen lds\n"
    "v_xor_b32 v96, s82, v13\n"
    "v_lshl_add_u32 v96, v96, 8, s84\n"
    "v_xor_b32 v97, v14, v13\n"
    "v_lshl_add_u32 v74, v97, 4, v96\n"
    "v_xor_b32 v75, 64, v74\n"
    "v_xor_b32 v76, 128, v74\n"
    "v_xor_b32 v77, 192, v74\n"
    "v_mov_b32 v60, 0x3f803f80\n"
    "s_add_u32 m0, s84, 5120\n"
    "s_nop 0\n"
    "buffer_load_dwordx4 v65, s[16:19], s88 offen lds\n"
    "v_mov_b32 v61, 0x3f803f80\n"
    "v_mov_b32 v62, 0x3f803f80\n"
    "v_mov_b32 v63, 0x3f803f80\n"
    "v_bfe_u32 v98, v12, 2, 2\n"
    "v_and_b32 v99, 3, v12\n"
    "v_lshlrev_b32 v99, 3, v99\n"
    "v_lshl_add_u32 v100, v14, 3, v98\n"
    "v_lshl_add_u32 v100, v100, 7, v99\n"
    "s_add_u32 m0, s84, 6144\n"
    "s_nop 0\n"
    "buffer_load_dwordx4 v66, s[16:19], s88 offen lds\n"
    "v_lshrrev_b32 v101, 1, v98\n"
    "v_and_b32 v102, 1, v14\n"
    "v_lshl_or_b32 v101, v102, 1, v101\n"
    "v_xor_b32 v103, s15, v101\n"
    "v_lshl_add_u32 v48, v103, 5, v100\n"
    "v_add_u32 v48, 65536, v48\n"
    "v_xor_b32 v49, 32, v48\n"
    "v_xor_b32 v50, 64, v48\n"
    "s_add_u32 m0, s84, 7168\n"
    "s_nop 0\n"
    "buffer_load_dwordx4 v67, s[16:19], s88 offen lds\n"
    "v_xor_b32 v51, 96, v48\n"
    "s_lshl_b32 s90, s15, 11\n"
    "s_add_u32 s90, s90, 149568\n"
    "v_lshl_add_u32 v82, v13, 7, s90\n"
    "v_lshl_add_u32 v82, v14, 3, v82\n"
    "v_add_u32 v84, s83, v13\n"
    "v_lshlrev_b32 v103, 3, v13\n"
    "v_lshl_add_u32 v83, v14, 14, v103\n"
    "s_xor_b32 s88, s85, 512\n"
    "s_add_u32 m0, s84, 8192\n"
    "s_nop 0\n"
    "buffer_load_dwordx4 v64, s[16:19], s88 offen lds\n"
    "s_lshl_b32 s90, s15, 7\n"
    "s_add_u32 s90, s90, 158864\n"
    "v_lshl_add_u32 v22, v13, 3, s90\n"
    "s_lshl_b32 s91, s76, 3\n"
    "v_add_u32 v85, s91, v22\n"
    "s_lshl_b32 s90, s15, 6\n"
    "v_lshlrev_b32 v103, 4, v14\n"
    "v_add_u32 v103, 159424, v103\n"
    "s_add_u32 m0, s84, 9216\n"
    "s_nop 0\n"
    "buffer_load_dwordx4 v65, s[16:19], s88 offen lds\n"
    "v_add_u32 v86, s90, v103\n"
    "s_xor_b32 s91, s90, 64\n"
    "v_add_u32 v88, s91, v103\n"
    "s_xor_b32 s91, s90, 128\n"
    "v_add_u32 v89, s91, v103\n"
    "s_xor_b32 s91, s90, 192\n"
    "v_add_u32 v90, s91, v103\n"
    "v_mov_b32 v10, 1.0\n"
    "s_add_u32 m0, s84, 10240\n"
    "s_nop 0\n"
    "buffer_load_dwordx4 v66, s[16:19], s88 offen lds\n"
    "v_lshl_add_u32 v98, v14, 2, v98\n"
    "v_lshl_add_u32 v100, v98, 7, v99\n"
    "v_lshl_add_u32 v28, v101, 5, v100\n"
    "v_add_u32 v28, 65536, v28\n"
    "v_xor_b32 v29, 32, v28\n"
    "v_xor_b32 v30, 64, v28\n"
    "v_xor_b32 v31, 96, v28\n"
    "v_mul_u32_u24 v98, 384, v98\n"
    "s_add_u32 m0, s84, 11264\n"
    "s_nop 0\n"
    "buffer_load_dwordx4 v67, s[16:19], s88 offen lds\n"
    "v_add_u32 v98, v98, v99\n"
    "s_mul_i32 s90, s15, 3\n"
    "s_bitcmp1_b32 s15, 0\n"
    "s_cselect_b32 s91, 1, 0\n"
    "s_add_u32 s91, s91, s90\n"
    "v_xor_b32 v103, s91, v101\n"
    "v_lshl_add_u32 v19, v103, 5, v98\n"
    "s_bitcmp1_b32 s15, 0\n"
    "s_cselect_b32 s91, 2, 1\n"
    "s_add_u32 s91, s91, s90\n"
    "s_xor_b32 s88, s85, 768\n"
    "s_add_u32 m0, s84, 12288\n"
    "s_nop 0\n"
    "buffer_load_dwordx4 v64, s[16:19], s88 offen lds\n"
    "v_xor_b32 v103, s91, v101\n"
    "v_lshl_add_u32 v20, v103, 5, v98\n"
    "s_bitcmp1_b32 s15, 0\n"
    "s_cselect_b32 s91, 0, 2\n"
    "s_add_u32 s91, s91, s90\n"
    "v_xor_b32 v103, s91, v101\n"
    "v_lshl_add_u32 v21, v103, 5, v98\n"
    "s_lshr_b32 s90, s36, 7\n"
    "s_sub_u32 s90, 3, s90\n"
    "s_and_b32 s90, s90, 7\n"
    "s_cmp_eq_u32 s14, 7\n"
    "s_cselect_b32 s91, s90, s14\n"
    "s_cmp_eq_u32 s14, s90\n"
    "s_cselect_b32 s91, 7, s91\n"
    "s_cmp_eq_u32 s91, 7\n"
    "s_cselect_b32 s91, 4, 64\n"
    "s_mul_i32 s90, s15, 192\n"
    "s_add_u32 m0, s84, 13312\n"
    "s_nop 0\n"
    "buffer_load_dwordx4 v65, s[16:19], s88 offen lds\n"
    "v_add_u32 v96, s90, v12\n"
    "v_mul_u32_u24 v97, 2731, v96\n"
    "v_lshrrev_b32 v97, 16, v97\n"
    "v_mul_u32_u24 v98, 24, v97\n"
    "v_sub_u32 v98, v96, v98\n"
    "v_and_b32 v99, 6, v97\n"
    "v_xor_b32 v98, v98, v99\n"
    "v_mul_u32_u24 v97, 2752, v97\n"
    "s_add_u32 m0, s84, 14336\n"
    "s_nop 0\n"
    "buffer_load_dwordx4 v66, s[16:19], s88 offen lds\n"
    "v_lshl_add_u32 v16, v98, 4, v97\n"
    "v_subrev_u32 v99, s91, v98\n"
    "v_ashrrev_i32 v99, 31, v99\n"
    "v_add_u32 v99, 1, v99\n"
    "v_lshl_add_u32 v16, v99, 30, v16\n"
    "v_add_u32 v96, s90, v12\n"
    "v_add_u32 v96, 64, v96\n"
    "v_mul_u32_u24 v97, 2731, v96\n"
    "s_add_u32 m0, s84, 15360\n"
    "s_nop 0\n"
    "buffer_load_dwordx4 v67, s[16:19], s88 offen lds\n"
    "v_lshrrev_b32 v97, 16, v97\n"
    "v_mul_u32_u24 v98, 24, v97\n"
    "v_sub_u32 v98, v96, v98\n"
    "v_and_b32 v99, 6, v97\n"
    "v_xor_b32 v98, v98, v99\n"
    "v_mul_u32_u24 v97, 2752, v97\n"
    "v_lshl_add_u32 v17, v98, 4, v97\n"
    "v_subrev_u32 v99, s91, v98\n"
    "s_lshl_b32 s88, s80, 14\n"
    "s_add_u32 s88, s88, s77\n"
    "s_lshl_b32 s89, s15, 11\n"
    "s_add_u32 m0, s89, 149568\n"
    "s_nop 0\n"
    "buffer_load_dwordx4 v72, s[16:19], s88 offen offset:0 lds\n"
    "v_ashrrev_i32 v99, 31, v99\n"
    "v_add_u32 v99, 1, v99\n"
    "v_lshl_add_u32 v17, v99, 30, v17\n"
    "v_subrev_u32 v17, 1024, v17\n"
    "v_add_u32 v96, s90, v12\n"
    "v_add_u32 v96, 128, v96\n"
    "v_mul_u32_u24 v97, 2731, v96\n"
    "v_lshrrev_b32 v97, 16, v97\n"
    "buffer_load_dwordx4 v73, s[16:19], s88 offen offset:1024 lds\n"
    "v_mul_u32_u24 v98, 24, v97\n"
    "v_sub_u32 v98, v96, v98\n"
    "v_and_b32 v99, 6, v97\n"
    "v_xor_b32 v98, v98, v99\n"
    "v_mul_u32_u24 v97, 2752, v97\n"
    "v_lshl_add_u32 v18, v98, 4, v97\n"
    "v_subrev_u32 v99, s91, v98\n"
    "v_ashrrev_i32 v99, 31, v99\n"
    "s_xor_b32 s88, s86, 131072\n"
    "s_add_u32 m0, s87, 81920\n"
    "s_nop 0\n"
    "buffer_load_dwordx4 v68, s[20:23], s88 offen offset:0 lds\n"
    "v_add_u32 v99, 1, v99\n"
    "v_lshl_add_u32 v18, v99, 30, v18\n"
    "v_subrev_u32 v18, 2048, v18\n"
    "v_and_b32 v24, 7, v12\n"
    "v_lshlrev_b32 v24, 7, v24\n"
    "v_lshlrev_b32 v26, 3, v13\n"
    "v_lshl_add_u32 v25, v14, 8, v26\n"
    "s_lshl_b32 s90, s15, 10\n"
    "buffer_load_dwordx4 v69, s[20:23], s88 offen offset:1024 lds\n"
    "s_add_u32 s90, s90, 65536\n"
    "v_add_u32 v27, s90, v0\n"
    "s_mov_b32 s48, 0x100001\n"
    "s_mov_b32 s49, 0x10000100\n"
    "s_mov_b32 s50, 0x200002\n"
    "s_mov_b32 s51, 0x20000200\n"
    "s_mov_b32 s52, 0x400004\n"
    "s_mov_b32 s53, 0x40000400\n"
    "buffer_load_dwordx4 v70, s[20:23], s88 offen offset:2048 lds\n"
    "s_mov_b32 s54, 0x800008\n"
    "s_mov_b32 s55, 0x80000800\n"
    "s_lshl_b32 s56, s14, 18\n"
    "s_lshl_b32 s90, s80, 7\n"
    "s_add_u32 s56, s56, s90\n"
    "s_add_u32 s56, s56, 1048576\n"
    "s_lshl_b32 s57, s14, 5\n"
    "s_add_u32 s57, s57, s13\n"
    "buffer_load_dwordx4 v71, s[20:23], s88 offen offset:3072 lds\n"
    "s_lshl_b32 s57, s57, 7\n"
    "s_add_u32 s57, s57, 65536\n"
    "s_lshl_b32 s70, s80, 14\n"
    "s_lshl_b32 s90, s14, 11\n"
    "s_add_u32 s70, s70, s90\n"
    "s_lshr_b32 s90, s15, 1\n"
    "s_lshl_b32 s90, s90, 10\n"
    "s_add_u32 s68, s70, s90\n"
    "s_add_u32 s68, s68, 3145728\n"
    "s_xor_b32 s69, s68, 1024\n"
    "s_add_u32 s70, s70, 5242880\n"
    "s_lshl_b32 s74, s80, 10\n"
    "s_add_u32 s74, s74, 196608\n"
    "s_add_u32 s72, s77, s74\n"
    "s_lshl_b32 s75, s13, 10\n"
    "s_add_u32 s75, s75, 131072\n"
    "s_add_u32 s73, s75, s77\n"
    "s_waitcnt vmcnt(22)\n"
    "s_waitcnt lgkmcnt(0)\n"
    "s_add_u32 s96, s96, 0x7f4a7c15\n"
    "s_addc_u32 s97, s97, 0x9e3779b9\n"
    "v_mov_b32 v4, s96\n"
    "v_mov_b32 v5, s97\n"
    "ds_read_b128 v[192:195], v74 offset:0\n"
    "ds_read_b128 v[196:199], v75 offset:0\n"
    "ds_read_b128 v[200:203], v76 offset:0\n"
    "ds_read_b128 v[204:207], v77 offset:0\n"
    "s_waitcnt vmcnt(18)\n"
    "ds_inc_u32 v2, v2\n"
    "ds_read_b32 v3, v1 offset:0\n"
    "s_xor_b32 s88, s86, 262144\n"
    "s_add_u32 m0, s87, 98304\n"
    "s_nop 0\n"
    "buffer_load_dwordx4 v68, s[20:23], s88 offen offset:0 lds\n"
    "buffer_load_dwordx4 v69, s[20:23], s88 offen offset:1024 lds\n"
    "buffer_load_dwordx4 v70, s[20:23], s88 offen offset:2048 lds\n"
    "buffer_load_dwordx4 v71, s[20:23], s88 offen offset:3072 lds\n"
    "s_waitcnt lgkmcnt(5)\n"
    "v_mfma_f32_16x16x32_bf16 v[124:127], v[60:63], v[192:195], 0\n"
    "v_mfma_f32_16x16x32_bf16 v[128:131], v[192:195], v[192:195], 0\n"
    "s_waitcnt lgkmcnt(4)\n"
    "v_mfma_f32_16x16x32_bf16 v[124:127], v[60:63], v[196:199], v[124:127]\n"
    "v_mfma_f32_16x16x32_bf16 v[128:131], v[196:199], v[196:199], v[128:131]\n"
    "s_waitcnt lgkmcnt(3)\n"
    "v_mfma_f32_16x16x32_bf16 v[124:127], v[60:63], v[200:203], v[124:127]\n"
    "v_mfma_f32_16x16x32_bf16 v[128:131], v[200:203], v[200:203], v[128:131]\n"
    "s_waitcnt lgkmcnt(2)\n"
    "v_mfma_f32_16x16x32_bf16 v[124:127], v[60:63], v[204:207], v[124:127]\n"
    "v_mfma_f32_16x16x32_bf16 v[128:131], v[204:207], v[204:207], v[128:131]\n"
    "s_waitcnt vmcnt(18)\n"
    "ds_read_b128 v[208:211], v74 offset:4096\n"
    "ds_read_b128 v[212:215], v75 offset:4096\n"
    "ds_read_b128 v[216:219], v76 offset:4096\n"
    "ds_read_b128 v[220:223], v77 offset:4096\n"
    "s_waitcnt lgkmcnt(4)\n"
    "v_cmp_gt_u32 vcc, 1, v3\n"
    "s_cbranch_vccnz .Ls4_cs1\n"
    ".Ls4_cb2:\n"
    "ds_read_b64_tr_b16 v[144:145], v48 offset:0\n"
    "ds_read_b64_tr_b16 v[146:147], v48 offset:512\n"
    "ds_read_b64_tr_b16 v[148:149], v49 offset:0\n"
    "ds_read_b64_tr_b16 v[150:151], v49 offset:512\n"
    "ds_read_b64_tr_b16 v[152:153], v50 offset:0\n"
    "ds_read_b64_tr_b16 v[154:155], v50 offset:512\n"
    "ds_read_b64_tr_b16 v[156:157], v51 offset:0\n"
    "ds_read_b64_tr_b16 v[158:159], v51 offset:512\n"
    "ds_read_b64_tr_b16 v[160:161], v48 offset:4096\n"
    "ds_read_b64_tr_b16 v[162:163], v48 offset:4608\n"
    "ds_read_b64_tr_b16 v[164:165], v49 offset:4096\n"
    "ds_read_b64_tr_b16 v[166:167], v49 offset:4608\n"
    "ds_read_b64_tr_b16 v[168:169], v50 offset:4096\n"
    "ds_read_b64_tr_b16 v[170:171], v50 offset:4608\n"
    "ds_read_b64_tr_b16 v[172:173], v51 offset:4096\n"
    "ds_read_b64_tr_b16 v[174:175], v51 offset:4608\n"
    "s_xor_b32 s88, s86, 393216\n"
    "s_add_u32 m0, s87, 114688\n"
    "s_nop 0\n"
    "buffer_load_dwordx4 v68, s[20:23], s88 offen offset:0 lds\n"
    "buffer_load_dwordx4 v69, s[20:23], s88 offen offset:1024 lds\n"
    "buffer_load_dwordx4 v70, s[20:23], s88 offen offset:2048 lds\n"
    "buffer_load_dwordx4 v71, s[20:23], s88 offen offset:3072 lds\n"
    "v_lshrrev_b32 v72, 3, v12\n"
    "v_and_b32 v73, 7, v12\n"
    "v_lshlrev_b32 v73, 4, v73\n"
    "v_lshl_add_u32 v73, v72, 10, v73\n"
    "v_lshlrev_b32 v72, 5, v14\n"
    "v_xor_b32 v64, v72, v73\n"
    "v_add_u32 v65, 7168, v64\n"
    "v_add_u32 v66, 14336, v64\n"
    "v_add_u32 v67, 21504, v64\n"
    "s_waitcnt vmcnt(18)\n"
    "ds_read_b128 v[224:227], v74 offset:8192\n"
    "ds_read_b128 v[228:231], v75 offset:8192\n"
    "ds_read_b128 v[232:235], v76 offset:8192\n"
    "ds_read_b128 v[236:239], v77 offset:8192\n"
    "s_waitcnt lgkmcnt(15)\n"
    "v_mfma_f32_16x16x32_bf16 v[132:135], v[144:147], v[60:63], 0\n"
    "v_mfma_f32_16x16x32_bf16 v[176:179], v[144:147], v[192:195], 0\n"
    "ds_read_b64_tr_b16 v[144:145], v48 offset:8192\n"
    "ds_read_b64_tr_b16 v[146:147], v48 offset:8704\n"
    "v_mfma_f32_16x16x32_bf16 v[180:183], v[148:151], v[192:195], 0\n"
    "ds_read_b64_tr_b16 v[148:149], v49 offset:8192\n"
    "ds_read_b64_tr_b16 v[150:151], v49 offset:8704\n"
    "s_waitcnt lgkmcnt(15)\n"
    "v_mfma_f32_16x16x32_bf16 v[184:187], v[152:155], v[192:195], 0\n"
    "ds_read_b64_tr_b16 v[152:153], v50 offset:8192\n"
    "ds_read_b64_tr_b16 v[154:155], v50 offset:8704\n"
    "v_mfma_f32_16x16x32_bf16 v[188:191], v[156:159], v[192:195], 0\n"
    "ds_read_b64_tr_b16 v[156:157], v51 offset:8192\n"
    "ds_read_b64_tr_b16 v[158:159], v51 offset:8704\n"
    "s_waitcnt lgkmcnt(15)\n"
    "v_mfma_f32_16x16x32_bf16 v[132:135], v[160:163], v[60:63], v[132:135]\n"
    "v_mfma_f32_16x16x32_bf16 v[176:179], v[160:163], v[196:199], v[176:179]\n"
    "ds_read_b64_tr_b16 v[160:161], v48 offset:12288\n"
    "ds_read_b64_tr_b16 v[162:163], v48 offset:12800\n"
    "v_mfma_f32_16x16x32_bf16 v[124:127], v[60:63], v[208:211], v[124:127]\n"
    "v_mfma_f32_16x16x32_bf16 v[128:131], v[208:211], v[208:211], v[128:131]\n"
    "v_mfma_f32_16x16x32_bf16 v[124:127], v[60:63], v[212:215], v[124:127]\n"
    "v_mfma_f32_16x16x32_bf16 v[128:131], v[212:215], v[212:215], v[128:131]\n"
    "v_mfma_f32_16x16x32_bf16 v[124:127], v[60:63], v[216:219], v[124:127]\n"
    "v_mfma_f32_16x16x32_bf16 v[128:131], v[216:219], v[216:219], v[128:131]\n"
    "v_mfma_f32_16x16x32_bf16 v[124:127], v[60:63], v[220:223], v[124:127]\n"
    "v_mfma_f32_16x16x32_bf16 v[128:131], v[220:223], v[220:223], v[128:131]\n"
    "s_waitcnt vmcnt(12)\n"
    "ds_read_b128 v[240:243], v74 offset:12288\n"
    "ds_read_b128 v[244:247], v75 offset:12288\n"
    "ds_read_b128 v[248:251], v76 offset:12288\n"
    "ds_read_b128 v[252:255], v77 offset:12288\n"
    "ds_read_b64 v[52:53], v82 offset:0\n"
    "ds_read_b64 v[54:55], v82 offset:32\n"
    "ds_read_b64 v[56:57], v82 offset:64\n"
    "ds_read_b64 v[58:59], v82 offset:96\n"
    "ds_inc_u32 v2, v2\n"
    "s_waitcnt lgkmcnt(15)\n"
    "v_mfma_f32_16x16x32_bf16 v[124:127], v[60:63], v[224:227], v[124:127]\n"
    "v_mfma_f32_16x16x32_bf16 v[128:131], v[224:227], v[224:227], v[128:131]\n"
    "s_waitcnt lgkmcnt(15)\n"
    "v_mfma_f32_16x16x32_bf16 v[124:127], v[60:63], v[228:231], v[124:127]\n"
    "v_mfma_f32_16x16x32_bf16 v[128:131], v[228:231], v[228:231], v[128:131]\n"
    "s_waitcnt lgkmcnt(15)\n"
    "v_mfma_f32_16x16x32_bf16 v[124:127], v[60:63], v[232:235], v[124:127]\n"
    "v_mfma_f32_16x16x32_bf16 v[128:131], v[232:235], v[232:235], v[128:131]\n"
    "s_waitcnt lgkmcnt(15)\n"
    "v_mfma_f32_16x16x32_bf16 v[124:127], v[60:63], v[236:239], v[124:127]\n"
    "v_mfma_f32_16x16x32_bf16 v[128:131], v[236:239], v[236:239], v[128:131]\n"
    "s_waitcnt lgkmcnt(8)\n"
    "v_mfma_f32_16x16x32_bf16 v[124:127], v[60:63], v[240:243], v[124:127]\n"
    "v_mfma_f32_16x16x32_bf16 v[128:131], v[240:243], v[240:243], v[128:131]\n"
    "s_waitcnt lgkmcnt(7)\n"
    "v_mfma_f32_16x16x32_bf16 v[124:127], v[60:63], v[244:247], v[124:127]\n"
    "v_mfma_f32_16x16x32_bf16 v[128:131], v[244:247], v[244:247], v[128:131]\n"
    "s_waitcnt lgkmcnt(6)\n"
    "v_mfma_f32_16x16x32_bf16 v[124:127], v[60:63], v[248:251], v[124:127]\n"
    "v_mfma_f32_16x16x32_bf16 v[128:131], v[248:251], v[248:251], v[128:131]\n"
    "s_waitcnt lgkmcnt(5)\n"
    "v_mfma_f32_16x16x32_bf16 v[124:127], v[60:63], v[252:255], v[124:127]\n"
    "v_mfma_f32_16x16x32_bf16 v[128:131], v[252:255], v[252:255], v[128:131]\n"
    "s_movk_i32 s61, 0x800\n"
    "s_waitcnt vmcnt(8)\n"
    "ds_inc_u32 v2, v2\n"
    "ds_read_b32 v3, v1 offset:0\n"
    "v_mfma_f32_16x16x32_bf16 v[180:183], v[164:167], v[196:199], v[180:183]\n"
    "ds_read_b64_tr_b16 v[164:165], v49 offset:12288\n"
    "ds_read_b64_tr_b16 v[166:167], v49 offset:12800\n"
    "s_nop 0\n"
    "v_cndmask_b32 v92, 0, v128, s[48:49]\n"
    "v_cndmask_b32 v92, v92, v129, s[50:51]\n"
    "v_cndmask_b32 v92, v92, v130, s[52:53]\n"
    "v_cndmask_b32 v92, v92, v131, s[54:55]\n"
    "s_nop 1\n"
    "v_mfma_f32_16x16x4_f32 v[128:131], v10, v92, 0\n"
    "v_mfma_f32_16x16x32_bf16 v[184:187], v[168:171], v[196:199], v[184:187]\n"
    "ds_read_b64_tr_b16 v[168:169], v50 offset:12288\n"
    "ds_read_b64_tr_b16 v[170:171], v50 offset:12800\n"
    "v_mul_f32 v124, 0x3b000000, v124\n"
    "s_nop 5\n"
    "v_mul_f32 v128, 0x3b000000, v128\n"
    "v_fma_f32 v128, -v124, v124, v128\n"
    "v_cvt_pk_bf16_f32 v93, v124, v124\n"
    "v_max_f32 v128, 0, v128\n"
    "v_mfma_f32_16x16x32_bf16 v[188:191], v[172:175], v[196:199], v[188:191]\n"
    "ds_read_b64_tr_b16 v[172:173], v51 offset:12288\n"
    "ds_read_b64_tr_b16 v[174:175], v51 offset:12800\n"
    "v_and_b32 v93, 0xffff0000, v93\n"
    "v_cvt_pk_bf16_f32 v95, v128, v128\n"
    "v_and_b32 v95, 0xffff0000, v95\n"
    "v_add_f32 v95, 0x3727c5ac, v95\n"
    "v_cvt_pk_bf16_f32 v95, v95, v95\n"
    "v_and_b32 v95, 0xffff0000, v95\n"
    "v_sqrt_f32 v95, v95\n"
    "s_waitcnt lgkmcnt(6)\n"
    "v_cmp_gt_u32 vcc, 3, v3\n"
    "s_cbranch_vccnz .Ls4_cs3\n"
    ".Ls4_cb4:\n"
    "v_mfma_f32_16x16x32_bf16 v[132:135], v[144:147], v[60:63], v[132:135]\n"
    "v_mfma_f32_16x16x32_bf16 v[176:179], v[144:147], v[200:203], v[176:179]\n"
    "ds_read_b64_tr_b16 v[144:145], v48 offset:16384\n"
    "ds_read_b64_tr_b16 v[146:147], v48 offset:16896\n"
    "v_cvt_pk_bf16_f32 v94, v95, v95\n"
    "v_and_b32 v94, 0xffff0000, v94\n"
    "v_rcp_f32 v96, v94\n"
    "v_sub_f32 v98, v124, v93\n"
    "v_mul_f32 v97, -v93, v96\n"
    "s_mov_b64 exec, 0xffff\n"
    "ds_write_b64 v85, v[96:97]\n"
    "s_mov_b64 exec, -1\n"
    "v_mfma_f32_16x16x32_bf16 v[180:183], v[148:151], v[200:203], v[180:183]\n"
    "ds_read_b64_tr_b16 v[148:149], v49 offset:16384\n"
    "ds_read_b64_tr_b16 v[150:151], v49 offset:16896\n"
    "ds_inc_u32 v2, v2\n"
    "ds_read_b32 v15, v1 offset:1024\n"
    "v_mul_f32 v98, v98, v96\n"
    "v_mul_f32 v99, v128, v96\n"
    "v_cvt_pk_bf16_f32 v98, v98, v98\n"
    "v_and_b32 v98, 0xffff0000, v98\n"
    "v_mfma_f32_16x16x32_bf16 v[184:187], v[152:155], v[200:203], v[184:187]\n"
    "ds_read_b64_tr_b16 v[152:153], v50 offset:16384\n"
    "ds_read_b64_tr_b16 v[154:155], v50 offset:16896\n"
    "v_mul_f32 v99, v99, v96\n"
    "v_fma_f32 v44, -v98, v94, -v93\n"
    "v_cvt_pk_bf16_f32 v99, v99, v99\n"
    "v_and_b32 v99, 0xffff0000, v99\n"
    "v_add_f32 v99, 0x3727c5ac, v99\n"
    "v_cvt_pk_bf16_f32 v99, v99, v99\n"
    "v_and_b32 v99, 0xffff0000, v99\n"
    "v_mfma_f32_16x16x32_bf16 v[188:191], v[156:159], v[200:203], v[188:191]\n"
    "ds_read_b64_tr_b16 v[156:157], v51 offset:16384\n"
    "ds_read_b64_tr_b16 v[158:159], v51 offset:16896\n"
    "v_sqrt_f32 v99, v99\n"
    "v_cmp_lt_u32 s[62:63], v84, s61\n"
    "v_cvt_pk_bf16_f32 v99, v99, v99\n"
    "v_and_b32 v99, 0xffff0000, v99\n"
    "v_mul_f32 v45, v99, v94\n"
    "v_rcp_f32 v98, v45\n"
    "s_waitcnt lgkmcnt(15)\n"
    "v_mfma_f32_16x16x32_bf16 v[132:135], v[160:163], v[60:63], v[132:135]\n"
    "v_mfma_f32_16x16x32_bf16 v[176:179], v[160:163], v[204:207], v[176:179]\n"
    "ds_read_b64_tr_b16 v[160:161], v48 offset:20480\n"
    "ds_read_b64_tr_b16 v[162:163], v48 offset:20992\n"
    "v_mov_b32 v99, 0x7f800000\n"
    "v_cndmask_b32 v98, 0, v98, s[62:63]\n"
    "v_cndmask_b32 v45, v99, v45, s[62:63]\n"
    "v_mul_f32 v47, 0xc0135761, v98\n"
    "v_mul_f32 v99, v98, v98\n"
    "v_mfma_f32_16x16x32_bf16 v[180:183], v[164:167], v[204:207], v[180:183]\n"
    "ds_read_b64_tr_b16 v[164:165], v49 offset:20480\n"
    "ds_read_b64_tr_b16 v[166:167], v49 offset:20992\n"
    "v_mul_f32 v99, 0x3d372713, v99\n"
    "v_mul_f32 v46, v47, v99\n"
    "s_waitcnt lgkmcnt(8)\n"
    "v_cmp_gt_u32 vcc, 1, v15\n"
    "s_cbranch_vccnz .Ls4_cs5\n"
    ".Ls4_cb6:\n"
    "ds_read_b32 v15, v1 offset:0\n"
    "v_lshlrev_b32 v100, 16, v52\n"
    "v_mfma_f32_16x16x32_bf16 v[184:187], v[168:171], v[204:207], v[184:187]\n"
    "ds_read_b64_tr_b16 v[168:169], v50 offset:20480\n"
    "ds_read_b64_tr_b16 v[170:171], v50 offset:20992\n"
    "v_and_b32 v101, 0xffff0000, v52\n"
    "v_lshlrev_b32 v102, 16, v53\n"
    "v_and_b32 v103, 0xffff0000, v53\n"
    "v_lshlrev_b32 v104, 16, v54\n"
    "v_and_b32 v105, 0xffff0000, v54\n"
    "v_mfma_f32_16x16x32_bf16 v[188:191], v[172:175], v[204:207], v[188:191]\n"
    "ds_read_b64_tr_b16 v[172:173], v51 offset:20480\n"
    "ds_read_b64_tr_b16 v[174:175], v51 offset:20992\n"
    "v_lshlrev_b32 v106, 16, v55\n"
    "v_and_b32 v107, 0xffff0000, v55\n"
    "v_lshlrev_b32 v108, 16, v56\n"
    "v_and_b32 v109, 0xffff0000, v56\n"
    "v_lshlrev_b32 v110, 16, v57\n"
    "v_mfma_f32_16x16x32_bf16 v[132:135], v[144:147], v[60:63], v[132:135]\n"
    "v_mfma_f32_16x16x32_bf16 v[176:179], v[144:147], v[208:211], v[176:179]\n"
    "ds_read_b64_tr_b16 v[144:145], v48 offset:24576\n"
    "ds_read_b64_tr_b16 v[146:147], v48 offset:25088\n"
    "v_and_b32 v111, 0xffff0000, v57\n"
    "v_lshlrev_b32 v112, 16, v58\n"
    "v_and_b32 v113, 0xffff0000, v58\n"
    "v_lshlrev_b32 v114, 16, v59\n"
    "v_and_b32 v115, 0xffff0000, v59\n"
    "v_mfma_f32_16x16x32_bf16 v[180:183], v[148:151], v[208:211], v[180:183]\n"
    "ds_read_b64_tr_b16 v[148:149], v49 offset:24576\n"
    "ds_read_b64_tr_b16 v[150:151], v49 offset:25088\n"
    "s_waitcnt lgkmcnt(8)\n"
    "v_cmp_gt_u32 vcc, 4, v15\n"
    "s_cbranch_vccnz .Ls4_cs7\n"
    ".Ls4_cb8:\n"
    "ds_read_b64 v[96:97], v22\n"
    "s_waitcnt lgkmcnt(0)\n"
    "v_fma_f32 v100, v100, v96, v97\n"
    "v_fma_f32 v101, v101, v96, v97\n"
    "v_mfma_f32_16x16x32_bf16 v[184:187], v[152:155], v[208:211], v[184:187]\n"
    "ds_read_b64_tr_b16 v[152:153], v50 offset:24576\n"
    "ds_read_b64_tr_b16 v[154:155], v50 offset:25088\n"
    "v_fma_f32 v102, v102, v96, v97\n"
    "v_fma_f32 v103, v103, v96, v97\n"
    "v_cvt_pk_bf16_f32 v52, v100, v101\n"
    "v_cvt_pk_bf16_f32 v53, v102, v103\n"
    "buffer_store_dwordx2 v[52:53], v83, s[24:27], s56 offen sc1\n"
    "v_mfma_f32_16x16x32_bf16 v[188:191], v[156:159], v[208:211], v[188:191]\n"
    "ds_read_b64_tr_b16 v[156:157], v51 offset:24576\n"
    "ds_read_b64_tr_b16 v[158:159], v51 offset:25088\n"
    "s_add_u32 s56, s56, 65536\n"
    "v_fma_f32 v104, v104, v96, v97\n"
    "v_fma_f32 v105, v105, v96, v97\n"
    "v_fma_f32 v106, v106, v96, v97\n"
    "v_fma_f32 v107, v107, v96, v97\n"
    "v_mfma_f32_16x16x32_bf16 v[132:135], v[160:163], v[60:63], v[132:135]\n"
    "v_mfma_f32_16x16x32_bf16 v[176:179], v[160:163], v[212:215], v[176:179]\n"
    "ds_read_b64_tr_b16 v[160:161], v48 offset:28672\n"
    "ds_read_b64_tr_b16 v[162:163], v48 offset:29184\n"
    "v_cvt_pk_bf16_f32 v54, v104, v105\n"
    "v_cvt_pk_bf16_f32 v55, v106, v107\n"
    "buffer_store_dwordx2 v[54:55], v83, s[24:27], s56 offen sc1\n"
    "s_add_u32 s56, s56, 65536\n"
    "v_fma_f32 v108, v108, v96, v97\n"
    "s_waitcnt vmcnt(6)\n"
    "ds_inc_u32 v2, v2\n"
    "ds_read_b32 v3, v1 offset:0\n"
    "v_mfma_f32_16x16x32_bf16 v[180:183], v[164:167], v[212:215], v[180:183]\n"
    "ds_read_b64_tr_b16 v[164:165], v49 offset:28672\n"
    "ds_read_b64_tr_b16 v[166:167], v49 offset:29184\n"
    "v_fma_f32 v109, v109, v96, v97\n"
    "v_fma_f32 v110, v110, v96, v97\n"
    "v_fma_f32 v111, v111, v96, v97\n"
    "v_cvt_pk_bf16_f32 v56, v108, v109\n"
    "v_cvt_pk_bf16_f32 v57, v110, v111\n"
    "v_mfma_f32_16x16x32_bf16 v[184:187], v[168:171], v[212:215], v[184:187]\n"
    "ds_read_b64_tr_b16 v[168:169], v50 offset:28672\n"
    "ds_read_b64_tr_b16 v[170:171], v50 offset:29184\n"
    "buffer_store_dwordx2 v[56:57], v83, s[24:27], s56 offen sc1\n"
    "s_add_u32 s56, s56, 65536\n"
    "v_fma_f32 v112, v112, v96, v97\n"
    "v_fma_f32 v113, v113, v96, v97\n"
    "v_fma_f32 v114, v114, v96, v97\n"
    "v_mfma_f32_16x16x32_bf16 v[188:191], v[172:175], v[212:215], v[188:191]\n"
    "ds_read_b64_tr_b16 v[172:173], v51 offset:28672\n"
    "ds_read_b64_tr_b16 v[174:175], v51 offset:29184\n"
    "v_fma_f32 v115, v115, v96, v97\n"
    "v_cvt_pk_bf16_f32 v58, v112, v113\n"
    "v_cvt_pk_bf16_f32 v59, v114, v115\n"
    "buffer_store_dwordx2 v[58:59], v83, s[24:27], s56 offen sc1\n"
    "s_waitcnt lgkmcnt(6)\n"
    "v_cmp_gt_u32 vcc, 5, v3\n"
    "s_cbranch_vccnz .Ls4_cs9\n"
    ".Ls4_cb10:\n"
    "v_mfma_f32_16x16x32_bf16 v[132:135], v[144:147], v[60:63], v[132:135]\n"
    "v_mfma_f32_16x16x32_bf16 v[176:179], v[144:147], v[216:219], v[176:179]\n"
    "ds_read_b64_tr_b16 v[144:145], v48 offset:32768\n"
    "ds_read_b64_tr_b16 v[146:147], v48 offset:33280\n"
    "v_mfma_f32_16x16x32_bf16 v[180:183], v[148:151], v[216:219], v[180:183]\n"
    "ds_read_b64_tr_b16 v[148:149], v49 offset:32768\n"
    "ds_read_b64_tr_b16 v[150:151], v49 offset:33280\n"
    "v_mfma_f32_16x16x32_bf16 v[184:187], v[152:155], v[216:219], v[184:187]\n"
    "ds_read_b64_tr_b16 v[152:153], v50 offset:32768\n"
    "ds_read_b64_tr_b16 v[154:155], v50 offset:33280\n"
    "v_mfma_f32_16x16x32_bf16 v[188:191], v[156:159], v[216:219], v[188:191]\n"
    "ds_read_b64_tr_b16 v[156:157], v51 offset:32768\n"
    "ds_read_b64_tr_b16 v[158:159], v51 offset:33280\n"
    "s_waitcnt lgkmcnt(12)\n"
    "v_mfma_f32_16x16x32_bf16 v[132:135], v[160:163], v[60:63], v[132:135]\n"
    "v_mfma_f32_16x16x32_bf16 v[176:179], v[160:163], v[220:223], v[176:179]\n"
    "ds_read_b64_tr_b16 v[160:161], v48 offset:36864\n"
    "ds_read_b64_tr_b16 v[162:163], v48 offset:37376\n"
    "v_mfma_f32_16x16x32_bf16 v[180:183], v[164:167], v[220:223], v[180:183]\n"
    "ds_read_b64_tr_b16 v[164:165], v49 offset:36864\n"
    "ds_read_b64_tr_b16 v[166:167], v49 offset:37376\n"
    "s_waitcnt lgkmcnt(12)\n"
    "v_mfma_f32_16x16x32_bf16 v[184:187], v[168:171], v[220:223], v[184:187]\n"
    "ds_read_b64_tr_b16 v[168:169], v50 offset:36864\n"
    "ds_read_b64_tr_b16 v[170:171], v50 offset:37376\n"
    "v_mfma_f32_16x16x32_bf16 v[188:191], v[172:175], v[220:223], v[188:191]\n"
    "ds_read_b64_tr_b16 v[172:173], v51 offset:36864\n"
    "ds_read_b64_tr_b16 v[174:175], v51 offset:37376\n"
    "s_waitcnt lgkmcnt(12)\n"
    "v_mfma_f32_16x16x32_bf16 v[132:135], v[144:147], v[60:63], v[132:135]\n"
    "v_mfma_f32_16x16x32_bf16 v[176:179], v[144:147], v[224:227], v[176:179]\n"
    "ds_read_b64_tr_b16 v[144:145], v48 offset:40960\n"
    "ds_read_b64_tr_b16 v[146:147], v48 offset:41472\n"
    "v_mfma_f32_16x16x32_bf16 v[180:183], v[148:151], v[224:227], v[180:183]\n"
    "ds_read_b64_tr_b16 v[148:149], v49 offset:40960\n"
    "ds_read_b64_tr_b16 v[150:151], v49 offset:41472\n"
    "s_waitcnt lgkmcnt(12)\n"
    "v_mfma_f32_16x16x32_bf16 v[184:187], v[152:155], v[224:227], v[184:187]\n"
    "ds_read_b64_tr_b16 v[152:153], v50 offset:40960\n"
    "ds_read_b64_tr_b16 v[154:155], v50 offset:41472\n"
    "v_mfma_f32_16x16x32_bf16 v[188:191], v[156:159], v[224:227], v[188:191]\n"
    "ds_read_b64_tr_b16 v[156:157], v51 offset:40960\n"
    "ds_read_b64_tr_b16 v[158:159], v51 offset:41472\n"
    "s_waitcnt lgkmcnt(12)\n"
    "v_mfma_f32_16x16x32_bf16 v[132:135], v[160:163], v[60:63], v[132:135]\n"
    "v_mfma_f32_16x16x32_bf16 v[176:179], v[160:163], v[228:231], v[176:179]\n"
    "ds_read_b64_tr_b16 v[160:161], v48 offset:45056\n"
    "ds_read_b64_tr_b16 v[162:163], v48 offset:45568\n"
    "s_waitcnt vmcnt(4)\n"
    "ds_inc_u32 v2, v2\n"
    "ds_read_b32 v3, v1 offset:0\n"
    "v_mfma_f32_16x16x32_bf16 v[180:183], v[164:167], v[228:231], v[180:183]\n"
    "ds_read_b64_tr_b16 v[164:165], v49 offset:45056\n"
    "ds_read_b64_tr_b16 v[166:167], v49 offset:45568\n"
    "s_waitcnt lgkmcnt(14)\n"
    "v_mfma_f32_16x16x32_bf16 v[184:187], v[168:171], v[228:231], v[184:187]\n"
    "ds_read_b64_tr_b16 v[168:169], v50 offset:45056\n"
    "ds_read_b64_tr_b16 v[170:171], v50 offset:45568\n"
    "v_mfma_f32_16x16x32_bf16 v[188:191], v[172:175], v[228:231], v[188:191]\n"
    "ds_read_b64_tr_b16 v[172:173], v51 offset:45056\n"
    "ds_read_b64_tr_b16 v[174:175], v51 offset:45568\n"
    "s_waitcnt lgkmcnt(6)\n"
    "v_cmp_gt_u32 vcc, 6, v3\n"
    "s_cbranch_vccnz .Ls4_cs11\n"
    ".Ls4_cb12:\n"
    "ds_inc_u32 v2, v2\n"
    "v_mfma_f32_16x16x32_bf16 v[132:135], v[144:147], v[60:63], v[132:135]\n"
    "v_mfma_f32_16x16x32_bf16 v[176:179], v[144:147], v[232:235], v[176:179]\n"
    "ds_read_b64_tr_b16 v[144:145], v48 offset:49152\n"
    "ds_read_b64_tr_b16 v[146:147], v48 offset:49664\n"
    "v_mfma_f32_16x16x32_bf16 v[180:183], v[148:151], v[232:235], v[180:183]\n"
    "ds_read_b64_tr_b16 v[148:149], v48 offset:53248\n"
    "ds_read_b64_tr_b16 v[150:151], v48 offset:53760\n"
    "v_mfma_f32_16x16x32_bf16 v[184:187], v[152:155], v[232:235], v[184:187]\n"
    "ds_read_b64_tr_b16 v[152:153], v48 offset:57344\n"
    "ds_read_b64_tr_b16 v[154:155], v48 offset:57856\n"
    "v_mfma_f32_16x16x32_bf16 v[188:191], v[156:159], v[232:235], v[188:191]\n"
    "ds_read_b64_tr_b16 v[156:157], v48 offset:61440\n"
    "ds_read_b64_tr_b16 v[158:159], v48 offset:61952\n"
    "s_waitcnt lgkmcnt(13)\n"
    "v_mfma_f32_16x16x32_bf16 v[132:135], v[160:163], v[60:63], v[132:135]\n"
    "v_mfma_f32_16x16x32_bf16 v[176:179], v[160:163], v[236:239], v[176:179]\n"
    "ds_read_b64_tr_b16 v[160:161], v49 offset:49152\n"
    "ds_read_b64_tr_b16 v[162:163], v49 offset:49664\n"
    "v_mfma_f32_16x16x32_bf16 v[180:183], v[164:167], v[236:239], v[180:183]\n"
    "ds_read_b64_tr_b16 v[164:165], v50 offset:49152\n"
    "ds_read_b64_tr_b16 v[166:167], v50 offset:49664\n"
    "s_waitcnt lgkmcnt(13)\n"
    "v_mfma_f32_16x16x32_bf16 v[184:187], v[168:171], v[236:239], v[184:187]\n"
    "ds_read_b64_tr_b16 v[168:169], v51 offset:49152\n"
    "ds_read_b64_tr_b16 v[170:171], v51 offset:49664\n"
    "v_mfma_f32_16x16x32_bf16 v[188:191], v[172:175], v[236:239], v[188:191]\n"
    "ds_read_b64_tr_b16 v[172:173], v49 offset:53248\n"
    "ds_read_b64_tr_b16 v[174:175], v49 offset:53760\n"
    "s_waitcnt lgkmcnt(12)\n"
    "v_mfma_f32_16x16x32_bf16 v[132:135], v[144:147], v[60:63], v[132:135]\n"
    "v_mfma_f32_16x16x32_bf16 v[176:179], v[144:147], v[240:243], v[176:179]\n"
    "ds_read_b64_tr_b16 v[144:145], v50 offset:53248\n"
    "ds_read_b64_tr_b16 v[146:147], v50 offset:53760\n"
    "v_mfma_f32_16x16x32_bf16 v[132:135], v[148:151], v[60:63], v[132:135]\n"
    "v_mfma_f32_16x16x32_bf16 v[176:179], v[148:151], v[244:247], v[176:179]\n"
    "ds_read_b64_tr_b16 v[148:149], v51 offset:53248\n"
    "ds_read_b64_tr_b16 v[150:151], v51 offset:53760\n"
    "s_waitcnt lgkmcnt(12)\n"
    "v_mfma_f32_16x16x32_bf16 v[132:135], v[152:155], v[60:63], v[132:135]\n"
    "v_mfma_f32_16x16x32_bf16 v[176:179], v[152:155], v[248:251], v[176:179]\n"
    "ds_read_b64_tr_b16 v[152:153], v49 offset:57344\n"
    "ds_read_b64_tr_b16 v[154:155], v49 offset:57856\n"
    "v_mfma_f32_16x16x32_bf16 v[132:135], v[156:159], v[60:63], v[132:135]\n"
    "v_mfma_f32_16x16x32_bf16 v[176:179], v[156:159], v[252:255], v[176:179]\n"
    "ds_read_b64_tr_b16 v[156:157], v50 offset:57344\n"
    "ds_read_b64_tr_b16 v[158:159], v50 offset:57856\n"
    "s_waitcnt lgkmcnt(12)\n"
    "v_mfma_f32_16x16x32_bf16 v[180:183], v[160:163], v[240:243], v[180:183]\n"
    "ds_read_b64_tr_b16 v[160:161], v51 offset:57344\n"
    "ds_read_b64_tr_b16 v[162:163], v51 offset:57856\n"
    "v_mfma_f32_16x16x32_bf16 v[184:187], v[164:167], v[240:243], v[184:187]\n"
    "ds_read_b64_tr_b16 v[164:165], v49 offset:61440\n"
    "ds_read_b64_tr_b16 v[166:167], v49 offset:61952\n"
    "s_mov_b32 exec_lo, 0x00010001\n"
    "s_mov_b32 exec_hi, 0x00010001\n"
    "ds_write_b128 v86, v[132:135]\n"
    "s_mov_b64 exec, -1\n"
    "ds_inc_u32 v2, v2\n"
    "s_waitcnt lgkmcnt(14)\n"
    "v_mfma_f32_16x16x32_bf16 v[188:191], v[168:171], v[240:243], v[188:191]\n"
    "ds_read_b64_tr_b16 v[168:169], v50 offset:61440\n"
    "ds_read_b64_tr_b16 v[170:171], v50 offset:61952\n"
    "v_mfma_f32_16x16x32_bf16 v[180:183], v[172:175], v[244:247], v[180:183]\n"
    "ds_read_b64_tr_b16 v[172:173], v51 offset:61440\n"
    "ds_read_b64_tr_b16 v[174:175], v51 offset:61952\n"
    "ds_inc_u32 v2, v2\n"
    "s_mov_b64 exec, 0xff\n"
    "s_waitcnt vmcnt(0)\n"
    "ds_inc_rtn_u32 v6, v11, v2 offset:0\n"
    "s_mov_b64 exec, -1\n"
    "s_waitcnt lgkmcnt(15)\n"
    "v_mfma_f32_16x16x32_bf16 v[184:187], v[144:147], v[244:247], v[184:187]\n"
    "v_mfma_f32_16x16x32_bf16 v[188:191], v[148:151], v[244:247], v[188:191]\n"
    "ds_read_b32 v3, v1 offset:0\n"
    "s_waitcnt lgkmcnt(13)\n"
    "v_mfma_f32_16x16x32_bf16 v[180:183], v[152:155], v[248:251], v[180:183]\n"
    "v_mfma_f32_16x16x32_bf16 v[184:187], v[156:159], v[248:251], v[184:187]\n"
    "s_waitcnt lgkmcnt(9)\n"
    "v_mfma_f32_16x16x32_bf16 v[188:191], v[160:163], v[248:251], v[188:191]\n"
    "s_waitcnt lgkmcnt(0)\n"
    "v_cmp_gt_u32 vcc, 8, v3\n"
    "s_cbranch_vccnz .Ls4_cs13\n"
    ".Ls4_cb14:\n"
    "ds_read_b128 v[104:107], v88\n"
    "ds_read_b128 v[108:111], v89\n"
    "ds_read_b128 v[112:115], v90\n"
    "v_mfma_f32_16x16x32_bf16 v[180:183], v[164:167], v[252:255], v[180:183]\n"
    "s_mov_b64 exec, 0xff\n"
    "v_cmpx_eq_u32 vcc, 3, v6\n"
    "buffer_store_dwordx4 v[4:7], v0, s[24:27], s57 offen sc1\n"
    "s_mov_b64 exec, -1\n"
    "s_nop 1\n"
    "v_mfma_f32_16x16x32_bf16 v[184:187], v[168:171], v[252:255], v[184:187]\n"
    "v_mfma_f32_16x16x32_bf16 v[188:191], v[172:175], v[252:255], v[188:191]\n"
    "v_mov_b32 v48, 160064\n"
    "ds_read_b32 v3, v48 offset:224\n"
    "ds_read_b32 v15, v1 offset:0\n"
    "s_waitcnt lgkmcnt(2)\n"
    "v_fmac_f32 v176, v132, v44\n"
    "v_fmac_f32 v177, v133, v44\n"
    "v_fmac_f32 v178, v134, v44\n"
    "v_fmac_f32 v179, v135, v44\n"
    "v_fmac_f32 v180, v104, v44\n"
    "v_fmac_f32 v181, v105, v44\n"
    "v_fmac_f32 v182, v106, v44\n"
    "v_fmac_f32 v183, v107, v44\n"
    "v_fmac_f32 v184, v108, v44\n"
    "v_fmac_f32 v185, v109, v44\n"
    "v_fmac_f32 v186, v110, v44\n"
    "v_fmac_f32 v187, v111, v44\n"
    "v_fmac_f32 v188, v112, v44\n"
    "v_fmac_f32 v189, v113, v44\n"
    "v_fmac_f32 v190, v114, v44\n"
    "v_fmac_f32 v191, v115, v44\n"
    "v_mul_f32 v208, v176, v176\n"
    "v_mul_f32 v209, v177, v177\n"
    "v_mul_f32 v210, v178, v178\n"
    "v_mul_f32 v211, v179, v179\n"
    "v_mul_f32 v212, v180, v180\n"
    "v_mul_f32 v213, v181, v181\n"
    "v_mul_f32 v214, v182, v182\n"
    "v_mul_f32 v215, v183, v183\n"
    "v_mul_f32 v216, v184, v184\n"
    "v_mul_f32 v217, v185, v185\n"
    "v_mul_f32 v218, v186, v186\n"
    "v_mul_f32 v219, v187, v187\n"
    "v_mul_f32 v220, v188, v188\n"
    "v_mul_f32 v221, v189, v189\n"
    "v_mul_f32 v222, v190, v190\n"
    "v_mul_f32 v223, v191, v191\n"
    "v_fma_f32 v208, v208, v46, v47\n"
    "v_fma_f32 v209, v209, v46, v47\n"
    "v_fma_f32 v210, v210, v46, v47\n"
    "v_fma_f32 v211, v211, v46, v47\n"
    "v_fma_f32 v212, v212, v46, v47\n"
    "v_fma_f32 v213, v213, v46, v47\n"
    "v_fma_f32 v214, v214, v46, v47\n"
    "v_fma_f32 v215, v215, v46, v47\n"
    "v_fma_f32 v216, v216, v46, v47\n"
    "v_fma_f32 v217, v217, v46, v47\n"
    "v_fma_f32 v218, v218, v46, v47\n"
    "v_fma_f32 v219, v219, v46, v47\n"
    "v_fma_f32 v220, v220, v46, v47\n"
    "v_fma_f32 v221, v221, v46, v47\n"
    "v_fma_f32 v222, v222, v46, v47\n"
    "v_fma_f32 v223, v223, v46, v47\n"
    "v_mul_f32 v208, v208, v176\n"
    "v_mul_f32 v209, v209, v177\n"
    "v_mul_f32 v210, v210, v178\n"
    "v_mul_f32 v211, v211, v179\n"
    "v_mul_f32 v212, v212, v180\n"
    "v_mul_f32 v213, v213, v181\n"
    "v_mul_f32 v214, v214, v182\n"
    "v_mul_f32 v215, v215, v183\n"
    "v_mul_f32 v216, v216, v184\n"
    "v_mul_f32 v217, v217, v185\n"
    "v_mul_f32 v218, v218, v186\n"
    "v_mul_f32 v219, v219, v187\n"
    "v_mul_f32 v220, v220, v188\n"
    "v_mul_f32 v221, v221, v189\n"
    "v_mul_f32 v222, v222, v190\n"
    "v_mul_f32 v223, v223, v191\n"
    "v_exp_f32 v208, v208\n"
    "v_exp_f32 v209, v209\n"
    "v_exp_f32 v210, v210\n"
    "v_exp_f32 v211, v211\n"
    "v_exp_f32 v212, v212\n"
    "v_exp_f32 v213, v213\n"
    "v_exp_f32 v214, v214\n"
    "v_exp_f32 v215, v215\n"
    "v_exp_f32 v216, v216\n"
    "v_exp_f32 v217, v217\n"
    "v_exp_f32 v218, v218\n"
    "v_exp_f32 v219, v219\n"
    "v_exp_f32 v220, v220\n"
    "v_exp_f32 v221, v221\n"
    "v_exp_f32 v222, v222\n"
    "v_exp_f32 v223, v223\n"
    "v_fma_f32 v208, v208, v45, v45\n"
    "v_fma_f32 v209, v209, v45, v45\n"
    "v_fma_f32 v210, v210, v45, v45\n"
    "v_fma_f32 v211, v211, v45, v45\n"
    "v_fma_f32 v212, v212, v45, v45\n"
    "v_fma_f32 v213, v213, v45, v45\n"
    "v_fma_f32 v214, v214, v45, v45\n"
    "v_fma_f32 v215, v215, v45, v45\n"
    "v_fma_f32 v216, v216, v45, v45\n"
    "v_fma_f32 v217, v217, v45, v45\n"
    "v_fma_f32 v218, v218, v45, v45\n"
    "v_fma_f32 v219, v219, v45, v45\n"
    "v_fma_f32 v220, v220, v45, v45\n"
    "v_fma_f32 v221, v221, v45, v45\n"
    "v_fma_f32 v222, v222, v45, v45\n"
    "v_fma_f32 v223, v223, v45, v45\n"
    "v_rcp_f32 v208, v208\n"
    "v_rcp_f32 v209, v209\n"
    "v_rcp_f32 v210, v210\n"
    "v_rcp_f32 v211, v211\n"
    "v_rcp_f32 v212, v212\n"
    "v_rcp_f32 v213, v213\n"
    "v_rcp_f32 v214, v214\n"
    "v_rcp_f32 v215, v215\n"
    "v_rcp_f32 v216, v216\n"
    "v_rcp_f32 v217, v217\n"
    "v_rcp_f32 v218, v218\n"
    "v_rcp_f32 v219, v219\n"
    "v_rcp_f32 v220, v220\n"
    "v_rcp_f32 v221, v221\n"
    "v_rcp_f32 v222, v222\n"
    "v_rcp_f32 v223, v223\n"
    "v_mul_f32 v176, v176, v208\n"
    "v_mul_f32 v177, v177, v209\n"
    "v_mul_f32 v178, v178, v210\n"
    "v_mul_f32 v179, v179, v211\n"
    "v_mul_f32 v180, v180, v212\n"
    "v_mul_f32 v181, v181, v213\n"
    "v_mul_f32 v182, v182, v214\n"
    "v_mul_f32 v183, v183, v215\n"
    "v_mul_f32 v184, v184, v216\n"
    "v_mul_f32 v185, v185, v217\n"
    "v_mul_f32 v186, v186, v218\n"
    "v_mul_f32 v187, v187, v219\n"
    "v_mul_f32 v188, v188, v220\n"
    "v_mul_f32 v189, v189, v221\n"
    "v_mul_f32 v190, v190, v222\n"
    "v_mul_f32 v191, v191, v223\n"
    "s_bitcmp1_b32 s15, 0\n"
    "s_cbranch_scc1 .Ls4_cv15\n"
    "v_cvt_pk_bf16_f32 v224, v176, v177\n"
    "v_cvt_pk_bf16_f32 v225, v178, v179\n"
    "v_cvt_pk_bf16_f32 v226, v180, v181\n"
    "v_cvt_pk_bf16_f32 v227, v182, v183\n"
    "v_cvt_pk_bf16_f32 v228, v184, v185\n"
    "v_cvt_pk_bf16_f32 v229, v186, v187\n"
    "v_cvt_pk_bf16_f32 v230, v188, v189\n"
    "v_cvt_pk_bf16_f32 v231, v190, v191\n"
    ".Ls4_cv16:\n"
    "buffer_store_dwordx4 v[224:227], v0, s[24:27], s68 offen sc1\n"
    "buffer_store_dwordx4 v[228:231], v0, s[24:27], s69 offen sc1\n"
    "s_lshr_b32 s83, s36, 7\n"
    "s_sub_u32 s83, 3, s83\n"
    "s_and_b32 s83, s83, 7\n"
    "s_cmp_eq_u32 s14, 7\n"
    "s_cselect_b32 s82, s83, s14\n"
    "s_cmp_eq_u32 s14, s83\n"
    "s_cselect_b32 s82, 7, s82\n"
    "s_mul_i32 s82, s82, 384\n"
    "s_mul_i32 s83, s15, 3072\n"
    "s_lshl_b32 s84, s14, 1\n"
    "s_and_b32 s89, s101, 1\n"
    "s_lshl_b32 s89, s89, 3\n"
    "s_xor_b32 s84, s84, s89\n"
    "s_waitcnt lgkmcnt(0)\n"
    "v_cmp_gt_u32 vcc, 9, v15\n"
    "s_cbranch_vccnz .Ls4_cs17\n"
    ".Ls4_cb18:\n"
    "s_add_u32 s94, s15, 0\n"
    "s_lshl_b32 s93, s14, 1\n"
    "s_xor_b32 s93, s94, s93\n"
    "s_lshl_b32 s94, s94, 12\n"
    "s_add_u32 m0, s94, 65536\n"
    "s_lshl_b32 s93, s93, 15\n"
    "s_add_u32 s93, s93, s77\n"
    "buffer_load_dwordx4 v64, s[28:31], s93 offen offset:0 lds\n"
    "buffer_load_dwordx4 v65, s[28:31], s93 offen offset:1024 lds\n"
    "buffer_load_dwordx4 v66, s[28:31], s93 offen offset:2048 lds\n"
    "buffer_load_dwordx4 v67, s[28:31], s93 offen offset:3072 lds\n"
    "s_add_u32 s94, s15, 4\n"
    "s_lshl_b32 s93, s14, 1\n"
    "s_xor_b32 s93, s94, s93\n"
    "s_lshl_b32 s94, s94, 12\n"
    "s_add_u32 m0, s94, 65536\n"
    "s_lshl_b32 s93, s93, 15\n"
    "s_add_u32 s93, s93, s77\n"
    "buffer_load_dwordx4 v64, s[28:31], s93 offen offset:0 lds\n"
    "buffer_load_dwordx4 v65, s[28:31], s93 offen offset:1024 lds\n"
    "s_mov_b64 exec, 0xff\n"
    "s_waitcnt vmcnt(6)\n"
    "buffer_store_dwordx4 v[4:7], v0, s[24:27], s72 offen sc1\n"
    "s_mov_b64 exec, -1\n"
    "buffer_load_dwordx4 v66, s[28:31], s93 offen offset:2048 lds\n"
    "buffer_load_dwordx4 v67, s[28:31], s93 offen offset:3072 lds\n"
    "s_add_u32 s94, s15, 8\n"
    "s_lshl_b32 s93, s14, 1\n"
    "s_xor_b32 s93, s94, s93\n"
    "s_lshl_b32 s94, s94, 12\n"
    "s_add_u32 m0, s94, 65536\n"
    "s_lshl_b32 s93, s93, 15\n"
    "s_add_u32 s93, s93, s77\n"
    "buffer_load_dwordx4 v64, s[28:31], s93 offen offset:0 lds\n"
    "buffer_load_dwordx4 v65, s[28:31], s93 offen offset:1024 lds\n"
    "buffer_load_dwordx4 v66, s[28:31], s93 offen offset:2048 lds\n"
    "buffer_load_dwordx4 v67, s[28:31], s93 offen offset:3072 lds\n"
    "s_add_u32 s94, s15, 12\n"
    "s_lshl_b32 s93, s14, 1\n"
    "s_xor_b32 s93, s94, s93\n"
    "s_lshl_b32 s94, s94, 12\n"
    "s_add_u32 m0, s94, 65536\n"
    "s_lshl_b32 s93, s93, 15\n"
    "s_add_u32 s93, s93, s77\n"
    "buffer_load_dwordx4 v64, s[28:31], s93 offen offset:0 lds\n"
    "buffer_load_dwordx4 v65, s[28:31], s93 offen offset:1024 lds\n"
    "buffer_load_dwordx4 v66, s[28:31], s93 offen offset:2048 lds\n"
    "buffer_load_dwordx4 v67, s[28:31], s93 offen offset:3072 lds\n"
    "v_cmp_gt_u32 vcc, 4, v3\n"
    "s_cbranch_vccnz .Ls4_ns19\n"
    ".Ls4_nb20:\n"
    "ds_read_b64_tr_b16 v[64:65], v19 offset:0\n"
    "ds_read_b64_tr_b16 v[66:67], v19 offset:6144\n"
    "ds_read_b64_tr_b16 v[68:69], v20 offset:0\n"
    "ds_read_b64_tr_b16 v[70:71], v20 offset:6144\n"
    "ds_read_b64_tr_b16 v[72:73], v21 offset:0\n"
    "ds_read_b64_tr_b16 v[74:75], v21 offset:6144\n"
    "ds_read_b64_tr_b16 v[76:77], v19 offset:12288\n"
    "ds_read_b64_tr_b16 v[78:79], v19 offset:18432\n"
    "ds_read_b64_tr_b16 v[80:81], v20 offset:12288\n"
    "ds_read_b64_tr_b16 v[82:83], v20 offset:18432\n"
    "ds_read_b64_tr_b16 v[84:85], v21 offset:12288\n"
    "ds_read_b64_tr_b16 v[86:87], v21 offset:18432\n"
    "ds_read_b64_tr_b16 v[88:89], v19 offset:24576\n"
    "ds_read_b64_tr_b16 v[90:91], v19 offset:30720\n"
    "ds_read_b64_tr_b16 v[92:93], v20 offset:24576\n"
    "ds_read_b64_tr_b16 v[94:95], v20 offset:30720\n"
    "ds_read_b64_tr_b16 v[96:97], v21 offset:24576\n"
    "ds_read_b64_tr_b16 v[98:99], v21 offset:30720\n"
    "ds_read_b64_tr_b16 v[100:101], v19 offset:36864\n"
    "ds_read_b64_tr_b16 v[102:103], v19 offset:43008\n"
    "ds_read_b64_tr_b16 v[104:105], v20 offset:36864\n"
    "ds_read_b64_tr_b16 v[106:107], v20 offset:43008\n"
    "ds_read_b64_tr_b16 v[108:109], v21 offset:36864\n"
    "ds_read_b64_tr_b16 v[110:111], v21 offset:43008\n"
    "ds_read_b64_tr_b16 v[112:113], v19 offset:49152\n"
    "ds_read_b64_tr_b16 v[114:115], v19 offset:55296\n"
    "ds_read_b64_tr_b16 v[116:117], v20 offset:49152\n"
    "ds_read_b64_tr_b16 v[118:119], v20 offset:55296\n"
    "ds_read_b64_tr_b16 v[120:121], v21 offset:49152\n"
    "ds_read_b64_tr_b16 v[122:123], v21 offset:55296\n"
    "ds_inc_u32 v2, v2\n"
    "ds_read_b32 v3, v1 offset:0\n"
    "s_lshl_b32 s90, s80, 14\n"
    "s_add_u32 s90, s90, 3145728\n"
    "s_lshl_b32 s91, s14, 11\n"
    "s_xor_b32 s48, s91, 0\n"
    "s_add_u32 s48, s48, s90\n"
    "s_xor_b32 s49, s91, 1024\n"
    "s_add_u32 s49, s49, s90\n"
    "s_xor_b32 s50, s91, 2048\n"
    "s_add_u32 s50, s50, s90\n"
    "s_xor_b32 s51, s91, 3072\n"
    "s_add_u32 s51, s51, s90\n"
    "s_xor_b32 s52, s91, 4096\n"
    "s_add_u32 s52, s52, s90\n"
    "s_xor_b32 s53, s91, 5120\n"
    "s_add_u32 s53, s53, s90\n"
    "s_xor_b32 s54, s91, 6144\n"
    "s_add_u32 s54, s54, s90\n"
    "s_xor_b32 s55, s91, 7168\n"
    "s_add_u32 s55, s55, s90\n"
    "s_xor_b32 s56, s91, 8192\n"
    "s_add_u32 s56, s56, s90\n"
    "s_xor_b32 s57, s91, 9216\n"
    "s_add_u32 s57, s57, s90\n"
    "s_xor_b32 s58, s91, 10240\n"
    "s_add_u32 s58, s58, s90\n"
    "s_xor_b32 s59, s91, 11264\n"
    "s_add_u32 s59, s59, s90\n"
    "s_xor_b32 s60, s91, 12288\n"
    "s_add_u32 s60, s60, s90\n"
    "s_xor_b32 s61, s91, 13312\n"
    "s_add_u32 s61, s61, s90\n"
    "s_xor_b32 s62, s91, 14336\n"
    "s_add_u32 s62, s62, s90\n"
    "s_xor_b32 s63, s91, 15360\n"
    "s_add_u32 s63, s63, s90\n"
    "s_lshl_b32 s64, s80, 10\n"
    "s_add_u32 s64, s64, 655360\n"
    "s_add_u32 s71, s64, s77\n"
    "s_lshl_b32 s90, s15, 2\n"
    "s_xor_b32 s90, s90, s84\n"
    "s_and_b32 s90, s90, 12\n"
    "s_lshl_b32 s90, s90, 10\n"
    "s_lshl_b32 s85, s13, 16\n"
    "s_add_u32 s85, s85, s90\n"
    "s_add_u32 s85, s85, 5242880\n"
    "s_lshl_b32 s87, s15, 12\n"
    "s_and_b32 s91, s14, 1\n"
    "s_lshl_b32 s91, s91, 11\n"
    "s_sub_u32 s86, s85, s91\n"
    "s_add_u32 s85, s85, s91\n"
    "s_waitcnt lgkmcnt(0)\n"
    "v_cmp_gt_u32 vcc, 10, v3\n"
    "s_cbranch_vccnz .Ls4_cs22\n"
    ".Ls4_cb23:\n"
    "s_xor_b32 s89, s84, 5\n"
    "s_mul_i32 s89, s89, 88064\n"
    "s_add_u32 m0, s83, 0\n"
    "s_add_u32 s89, s89, s82\n"
    "buffer_load_dwordx4 v16, s[32:35], s89 offen offset:0 lds\n"
    "buffer_load_dwordx4 v17, s[32:35], s89 offen offset:1024 lds\n"
    "buffer_load_dwordx4 v18, s[32:35], s89 offen offset:2048 lds\n"
    "s_xor_b32 s89, s84, 6\n"
    "s_mul_i32 s89, s89, 88064\n"
    "s_add_u32 m0, s83, 12288\n"
    "s_add_u32 s89, s89, s82\n"
    "buffer_load_dwordx4 v16, s[32:35], s89 offen offset:0 lds\n"
    "buffer_load_dwordx4 v17, s[32:35], s89 offen offset:1024 lds\n"
    "buffer_load_dwordx4 v18, s[32:35], s89 offen offset:2048 lds\n"
    "s_xor_b32 s89, s84, 7\n"
    "s_mul_i32 s89, s89, 88064\n"
    "s_add_u32 m0, s83, 24576\n"
    "s_add_u32 s89, s89, s82\n"
    "buffer_load_dwordx4 v16, s[32:35], s89 offen offset:0 lds\n"
    "buffer_load_dwordx4 v17, s[32:35], s89 offen offset:1024 lds\n"
    "buffer_load_dwordx2 v[6:7], v24, s[24:27], s74 offen sc1\n"
    "s_waitcnt vmcnt(9)\n"
    "ds_inc_u32 v2, v2\n"
    "ds_read_b32 v15, v1 offset:0\n"
    "v_lshlrev_b32 v176, 16, v52\n"
    "v_and_b32 v177, 0xffff0000, v52\n"
    "v_lshlrev_b32 v178, 16, v53\n"
    "v_and_b32 v179, 0xffff0000, v53\n"
    "v_lshlrev_b32 v180, 16, v54\n"
    "v_and_b32 v181, 0xffff0000, v54\n"
    "v_lshlrev_b32 v182, 16, v55\n"
    "v_and_b32 v183, 0xffff0000, v55\n"
    "v_lshlrev_b32 v184, 16, v56\n"
    "v_and_b32 v185, 0xffff0000, v56\n"
    "v_lshlrev_b32 v186, 16, v57\n"
    "v_and_b32 v187, 0xffff0000, v57\n"
    "v_lshlrev_b32 v188, 16, v58\n"
    "v_and_b32 v189, 0xffff0000, v58\n"
    "v_lshlrev_b32 v190, 16, v59\n"
    "v_and_b32 v191, 0xffff0000, v59\n"
    "v_mfma_f32_16x16x32_bf16 v[32:35], v[64:67], v[60:63], 0\n"
    "v_mfma_f32_16x16x32_bf16 v[36:39], v[68:71], v[60:63], 0\n"
    "v_mfma_f32_16x16x32_bf16 v[40:43], v[72:75], v[60:63], 0\n"
    "v_mfma_f32_16x16x32_bf16 v[32:35], v[76:79], v[60:63], v[32:35]\n"
    "v_mfma_f32_16x16x32_bf16 v[36:39], v[80:83], v[60:63], v[36:39]\n"
    "v_mfma_f32_16x16x32_bf16 v[40:43], v[84:87], v[60:63], v[40:43]\n"
    "v_mfma_f32_16x16x32_bf16 v[32:35], v[88:91], v[60:63], v[32:35]\n"
    "v_mfma_f32_16x16x32_bf16 v[36:39], v[92:95], v[60:63], v[36:39]\n"
    "v_mfma_f32_16x16x32_bf16 v[40:43], v[96:99], v[60:63], v[40:43]\n"
    "v_mfma_f32_16x16x32_bf16 v[32:35], v[100:103], v[60:63], v[32:35]\n"
    "v_mfma_f32_16x16x32_bf16 v[36:39], v[104:107], v[60:63], v[36:39]\n"
    "v_mfma_f32_16x16x32_bf16 v[40:43], v[108:111], v[60:63], v[40:43]\n"
    "v_mfma_f32_16x16x32_bf16 v[32:35], v[112:115], v[60:63], v[32:35]\n"
    "v_mfma_f32_16x16x32_bf16 v[36:39], v[116:119], v[60:63], v[36:39]\n"
    "v_mfma_f32_16x16x32_bf16 v[40:43], v[120:123], v[60:63], v[40:43]\n"
    "s_waitcnt vmcnt(0)\n"
    "v_cmp_ne_u64 vcc, s[96:97], v[6:7]\n"
    "s_cbranch_vccnz .Ls4_ps24\n"
    ".Ls4_pb25:\n"
    "buffer_load_dwordx4 v[192:195], v0, s[24:27], s48 offen sc1\n"
    "buffer_load_dwordx4 v[196:199], v0, s[24:27], s49 offen sc1\n"
    "buffer_load_dwordx4 v[200:203], v0, s[24:27], s50 offen sc1\n"
    "buffer_load_dwordx4 v[204:207], v0, s[24:27], s51 offen sc1\n"
    "buffer_load_dwordx4 v[208:211], v0, s[24:27], s52 offen sc1\n"
    "buffer_load_dwordx4 v[212:215], v0, s[24:27], s53 offen sc1\n"
    "buffer_load_dwordx4 v[216:219], v0, s[24:27], s54 offen sc1\n"
    "buffer_load_dwordx4 v[220:223], v0, s[24:27], s55 offen sc1\n"
    "buffer_load_dwordx4 v[224:227], v0, s[24:27], s56 offen sc1\n"
    "buffer_load_dwordx4 v[228:231], v0, s[24:27], s57 offen sc1\n"
    "buffer_load_dwordx4 v[232:235], v0, s[24:27], s58 offen sc1\n"
    "buffer_load_dwordx4 v[236:239], v0, s[24:27], s59 offen sc1\n"
    "buffer_load_dwordx4 v[240:243], v0, s[24:27], s60 offen sc1\n"
    "buffer_load_dwordx4 v[244:247], v0, s[24:27], s61 offen sc1\n"
    "buffer_load_dwordx4 v[248:251], v0, s[24:27], s62 offen sc1\n"
    "buffer_load_dwordx4 v[252:255], v0, s[24:27], s63 offen sc1\n"
    "s_waitcnt lgkmcnt(0)\n"
    "v_cmp_gt_u32 vcc, 11, v15\n"
    "s_cbranch_vccnz .Ls4_cs26\n"
    ".Ls4_cb27:\n"
    "ds_read_b64_tr_b16 v[128:129], v28 offset:0\n"
    "ds_read_b64_tr_b16 v[130:131], v28 offset:2048\n"
    "ds_read_b64_tr_b16 v[132:133], v29 offset:0\n"
    "ds_read_b64_tr_b16 v[134:135], v29 offset:2048\n"
    "ds_read_b64_tr_b16 v[136:137], v30 offset:0\n"
    "ds_read_b64_tr_b16 v[138:139], v30 offset:2048\n"
    "ds_read_b64_tr_b16 v[140:141], v31 offset:0\n"
    "ds_read_b64_tr_b16 v[142:143], v31 offset:2048\n"
    "ds_read_b64_tr_b16 v[144:145], v28 offset:4096\n"
    "ds_read_b64_tr_b16 v[146:147], v28 offset:6144\n"
    "ds_read_b64_tr_b16 v[148:149], v29 offset:4096\n"
    "ds_read_b64_tr_b16 v[150:151], v29 offset:6144\n"
    "ds_read_b64_tr_b16 v[152:153], v30 offset:4096\n"
    "ds_read_b64_tr_b16 v[154:155], v30 offset:6144\n"
    "ds_read_b64_tr_b16 v[156:157], v31 offset:4096\n"
    "ds_read_b64_tr_b16 v[158:159], v31 offset:6144\n"
    "ds_read_b64_tr_b16 v[160:161], v28 offset:8192\n"
    "ds_read_b64_tr_b16 v[162:163], v28 offset:10240\n"
    "ds_read_b64_tr_b16 v[164:165], v29 offset:8192\n"
    "ds_read_b64_tr_b16 v[166:167], v29 offset:10240\n"
    "ds_read_b64_tr_b16 v[168:169], v30 offset:8192\n"
    "ds_read_b64_tr_b16 v[170:171], v30 offset:10240\n"
    "ds_read_b64_tr_b16 v[172:173], v31 offset:8192\n"
    "ds_read_b64_tr_b16 v[174:175], v31 offset:10240\n"
    "s_waitcnt vmcnt(15)\n"
    "s_waitcnt lgkmcnt(15)\n"
    "v_mfma_f32_16x16x32_bf16 v[176:179], v[128:131], v[192:195], v[176:179]\n"
    "ds_read_b64_tr_b16 v[128:129], v28 offset:12288\n"
    "ds_read_b64_tr_b16 v[130:131], v28 offset:14336\n"
    "buffer_load_dwordx4 v18, s[32:35], s89 offen offset:2048 lds\n"
    "v_mfma_f32_16x16x32_bf16 v[180:183], v[132:135], v[192:195], v[180:183]\n"
    "ds_read_b64_tr_b16 v[132:133], v29 offset:12288\n"
    "ds_read_b64_tr_b16 v[134:135], v29 offset:14336\n"
    "s_waitcnt lgkmcnt(15)\n"
    "v_mfma_f32_16x16x32_bf16 v[184:187], v[136:139], v[192:195], v[184:187]\n"
    "ds_read_b64_tr_b16 v[136:137], v30 offset:12288\n"
    "ds_read_b64_tr_b16 v[138:139], v30 offset:14336\n"
    "v_mfma_f32_16x16x32_bf16 v[188:191], v[140:143], v[192:195], v[188:191]\n"
    "ds_read_b64_tr_b16 v[140:141], v31 offset:12288\n"
    "ds_read_b64_tr_b16 v[142:143], v31 offset:14336\n"
    "s_waitcnt vmcnt(15)\n"
    "s_waitcnt lgkmcnt(15)\n"
    "v_mfma_f32_16x16x32_bf16 v[176:179], v[144:147], v[196:199], v[176:179]\n"
    "ds_read_b64_tr_b16 v[144:145], v28 offset:16384\n"
    "ds_read_b64_tr_b16 v[146:147], v28 offset:18432\n"
    "s_xor_b32 s89, s84, 8\n"
    "s_mul_i32 s89, s89, 88064\n"
    "s_add_u32 m0, s83, 36864\n"
    "s_add_u32 s89, s89, s82\n"
    "buffer_load_dwordx4 v16, s[32:35], s89 offen offset:0 lds\n"
    "v_mfma_f32_16x16x32_bf16 v[180:183], v[148:151], v[196:199], v[180:183]\n"
    "ds_read_b64_tr_b16 v[148:149], v29 offset:16384\n"
    "ds_read_b64_tr_b16 v[150:151], v29 offset:18432\n"
    "s_waitcnt lgkmcnt(15)\n"
    "v_mfma_f32_16x16x32_bf16 v[184:187], v[152:155], v[196:199], v[184:187]\n"
    "ds_read_b64_tr_b16 v[152:153], v30 offset:16384\n"
    "ds_read_b64_tr_b16 v[154:155], v30 offset:18432\n"
    "v_mfma_f32_16x16x32_bf16 v[188:191], v[156:159], v[196:199], v[188:191]\n"
    "ds_read_b64_tr_b16 v[156:157], v31 offset:16384\n"
    "ds_read_b64_tr_b16 v[158:159], v31 offset:18432\n"
    "s_waitcnt vmcnt(15)\n"
    "s_waitcnt lgkmcnt(15)\n"
    "v_mfma_f32_16x16x32_bf16 v[176:179], v[160:163], v[200:203], v[176:179]\n"
    "ds_read_b64_tr_b16 v[160:161], v28 offset:20480\n"
    "ds_read_b64_tr_b16 v[162:163], v28 offset:22528\n"
    "buffer_load_dwordx4 v17, s[32:35], s89 offen offset:1024 lds\n"
    "v_mfma_f32_16x16x32_bf16 v[180:183], v[164:167], v[200:203], v[180:183]\n"
    "ds_read_b64_tr_b16 v[164:165], v29 offset:20480\n"
    "ds_read_b64_tr_b16 v[166:167], v29 offset:22528\n"
    "s_waitcnt lgkmcnt(15)\n"
    "v_mfma_f32_16x16x32_bf16 v[184:187], v[168:171], v[200:203], v[184:187]\n"
    "ds_read_b64_tr_b16 v[168:169], v30 offset:20480\n"
    "ds_read_b64_tr_b16 v[170:171], v30 offset:22528\n"
    "v_mfma_f32_16x16x32_bf16 v[188:191], v[172:175], v[200:203], v[188:191]\n"
    "ds_read_b64_tr_b16 v[172:173], v31 offset:20480\n"
    "ds_read_b64_tr_b16 v[174:175], v31 offset:22528\n"
    "s_waitcnt vmcnt(15)\n"
    "s_waitcnt lgkmcnt(15)\n"
    "v_mfma_f32_16x16x32_bf16 v[176:179], v[128:131], v[204:207], v[176:179]\n"
    "ds_read_b64_tr_b16 v[128:129], v28 offset:24576\n"
    "ds_read_b64_tr_b16 v[130:131], v28 offset:26624\n"
    "buffer_load_dwordx4 v18, s[32:35], s89 offen offset:2048 lds\n"
    "v_mfma_f32_16x16x32_bf16 v[180:183], v[132:135], v[204:207], v[180:183]\n"
    "ds_read_b64_tr_b16 v[132:133], v29 offset:24576\n"
    "ds_read_b64_tr_b16 v[134:135], v29 offset:26624\n"
    "s_waitcnt lgkmcnt(15)\n"
    "v_mfma_f32_16x16x32_bf16 v[184:187], v[136:139], v[204:207], v[184:187]\n"
    "ds_read_b64_tr_b16 v[136:137], v30 offset:24576\n"
    "ds_read_b64_tr_b16 v[138:139], v30 offset:26624\n"
    "v_mfma_f32_16x16x32_bf16 v[188:191], v[140:143], v[204:207], v[188:191]\n"
    "ds_read_b64_tr_b16 v[140:141], v31 offset:24576\n"
    "ds_read_b64_tr_b16 v[142:143], v31 offset:26624\n"
    "s_waitcnt vmcnt(15)\n"
    "s_waitcnt lgkmcnt(15)\n"
    "v_mfma_f32_16x16x32_bf16 v[176:179], v[144:147], v[208:211], v[176:179]\n"
    "ds_read_b64_tr_b16 v[144:145], v28 offset:28672\n"
    "ds_read_b64_tr_b16 v[146:147], v28 offset:30720\n"
    "s_xor_b32 s89, s84, 9\n"
    "s_mul_i32 s89, s89, 88064\n"
    "s_add_u32 m0, s83, 49152\n"
    "s_add_u32 s89, s89, s82\n"
    "buffer_load_dwordx4 v16, s[32:35], s89 offen offset:0 lds\n"
    "v_mfma_f32_16x16x32_bf16 v[180:183], v[148:151], v[208:211], v[180:183]\n"
    "ds_read_b64_tr_b16 v[148:149], v29 offset:28672\n"
    "ds_read_b64_tr_b16 v[150:151], v29 offset:30720\n"
    "s_waitcnt lgkmcnt(15)\n"
    "v_mfma_f32_16x16x32_bf16 v[184:187], v[152:155], v[208:211], v[184:187]\n"
    "ds_read_b64_tr_b16 v[152:153], v30 offset:28672\n"
    "ds_read_b64_tr_b16 v[154:155], v30 offset:30720\n"
    "v_mfma_f32_16x16x32_bf16 v[188:191], v[156:159], v[208:211], v[188:191]\n"
    "ds_read_b64_tr_b16 v[156:157], v31 offset:28672\n"
    "ds_read_b64_tr_b16 v[158:159], v31 offset:30720\n"
    "s_waitcnt vmcnt(15)\n"
    "s_waitcnt lgkmcnt(15)\n"
    "v_mfma_f32_16x16x32_bf16 v[176:179], v[160:163], v[212:215], v[176:179]\n"
    "ds_read_b64_tr_b16 v[160:161], v28 offset:32768\n"
    "ds_read_b64_tr_b16 v[162:163], v28 offset:34816\n"
    "buffer_load_dwordx4 v17, s[32:35], s89 offen offset:1024 lds\n"
    "v_mfma_f32_16x16x32_bf16 v[180:183], v[164:167], v[212:215], v[180:183]\n"
    "ds_read_b64_tr_b16 v[164:165], v29 offset:32768\n"
    "ds_read_b64_tr_b16 v[166:167], v29 offset:34816\n"
    "s_waitcnt lgkmcnt(15)\n"
    "v_mfma_f32_16x16x32_bf16 v[184:187], v[168:171], v[212:215], v[184:187]\n"
    "ds_read_b64_tr_b16 v[168:169], v30 offset:32768\n"
    "ds_read_b64_tr_b16 v[170:171], v30 offset:34816\n"
    "v_mfma_f32_16x16x32_bf16 v[188:191], v[172:175], v[212:215], v[188:191]\n"
    "ds_read_b64_tr_b16 v[172:173], v31 offset:32768\n"
    "ds_read_b64_tr_b16 v[174:175], v31 offset:34816\n"
    "s_waitcnt vmcnt(15)\n"
    "s_waitcnt lgkmcnt(15)\n"
    "v_mfma_f32_16x16x32_bf16 v[176:179], v[128:131], v[216:219], v[176:179]\n"
    "ds_read_b64_tr_b16 v[128:129], v28 offset:36864\n"
    "ds_read_b64_tr_b16 v[130:131], v28 offset:38912\n"
    "buffer_load_dwordx4 v18, s[32:35], s89 offen offset:2048 lds\n"
    "v_mfma_f32_16x16x32_bf16 v[180:183], v[132:135], v[216:219], v[180:183]\n"
    "ds_read_b64_tr_b16 v[132:133], v29 offset:36864\n"
    "ds_read_b64_tr_b16 v[134:135], v29 offset:38912\n"
    "s_waitcnt lgkmcnt(15)\n"
    "v_mfma_f32_16x16x32_bf16 v[184:187], v[136:139], v[216:219], v[184:187]\n"
    "ds_read_b64_tr_b16 v[136:137], v30 offset:36864\n"
    "ds_read_b64_tr_b16 v[138:139], v30 offset:38912\n"
    "v_mfma_f32_16x16x32_bf16 v[188:191], v[140:143], v[216:219], v[188:191]\n"
    "ds_read_b64_tr_b16 v[140:141], v31 offset:36864\n"
    "ds_read_b64_tr_b16 v[142:143], v31 offset:38912\n"
    "s_waitcnt vmcnt(15)\n"
    "s_waitcnt lgkmcnt(15)\n"
    "v_mfma_f32_16x16x32_bf16 v[176:179], v[144:147], v[220:223], v[176:179]\n"
    "ds_read_b64_tr_b16 v[144:145], v28 offset:40960\n"
    "ds_read_b64_tr_b16 v[146:147], v28 offset:43008\n"
    "v_mfma_f32_16x16x32_bf16 v[180:183], v[148:151], v[220:223], v[180:183]\n"
    "ds_read_b64_tr_b16 v[148:149], v29 offset:40960\n"
    "ds_read_b64_tr_b16 v[150:151], v29 offset:43008\n"
    "s_waitcnt lgkmcnt(15)\n"
    "v_mfma_f32_16x16x32_bf16 v[184:187], v[152:155], v[220:223], v[184:187]\n"
    "ds_read_b64_tr_b16 v[152:153], v30 offset:40960\n"
    "ds_read_b64_tr_b16 v[154:155], v30 offset:43008\n"
    "v_mfma_f32_16x16x32_bf16 v[188:191], v[156:159], v[220:223], v[188:191]\n"
    "ds_read_b64_tr_b16 v[156:157], v31 offset:40960\n"
    "ds_read_b64_tr_b16 v[158:159], v31 offset:43008\n"
    "s_waitcnt vmcnt(14)\n"
    "s_waitcnt lgkmcnt(15)\n"
    "v_mfma_f32_16x16x32_bf16 v[176:179], v[160:163], v[224:227], v[176:179]\n"
    "ds_read_b64_tr_b16 v[160:161], v28 offset:45056\n"
    "ds_read_b64_tr_b16 v[162:163], v28 offset:47104\n"
    "v_mfma_f32_16x16x32_bf16 v[180:183], v[164:167], v[224:227], v[180:183]\n"
    "ds_read_b64_tr_b16 v[164:165], v29 offset:45056\n"
    "ds_read_b64_tr_b16 v[166:167], v29 offset:47104\n"
    "s_waitcnt lgkmcnt(15)\n"
    "v_mfma_f32_16x16x32_bf16 v[184:187], v[168:171], v[224:227], v[184:187]\n"
    "ds_read_b64_tr_b16 v[168:169], v30 offset:45056\n"
    "ds_read_b64_tr_b16 v[170:171], v30 offset:47104\n"
    "v_mfma_f32_16x16x32_bf16 v[188:191], v[172:175], v[224:227], v[188:191]\n"
    "ds_read_b64_tr_b16 v[172:173], v31 offset:45056\n"
    "ds_read_b64_tr_b16 v[174:175], v31 offset:47104\n"
    "s_waitcnt vmcnt(13)\n"
    "s_waitcnt lgkmcnt(15)\n"
    "v_mfma_f32_16x16x32_bf16 v[176:179], v[128:131], v[228:231], v[176:179]\n"
    "ds_read_b64_tr_b16 v[128:129], v28 offset:49152\n"
    "ds_read_b64_tr_b16 v[130:131], v28 offset:51200\n"
    "v_mfma_f32_16x16x32_bf16 v[180:183], v[132:135], v[228:231], v[180:183]\n"
    "ds_read_b64_tr_b16 v[132:133], v29 offset:49152\n"
    "ds_read_b64_tr_b16 v[134:135], v29 offset:51200\n"
    "s_waitcnt lgkmcnt(15)\n"
    "v_mfma_f32_16x16x32_bf16 v[184:187], v[136:139], v[228:231], v[184:187]\n"
    "ds_read_b64_tr_b16 v[136:137], v30 offset:49152\n"
    "ds_read_b64_tr_b16 v[138:139], v30 offset:51200\n"
    "v_mfma_f32_16x16x32_bf16 v[188:191], v[140:143], v[228:231], v[188:191]\n"
    "ds_read_b64_tr_b16 v[140:141], v31 offset:49152\n"
    "ds_read_b64_tr_b16 v[142:143], v31 offset:51200\n"
    "s_waitcnt vmcnt(12)\n"
    "s_waitcnt lgkmcnt(15)\n"
    "v_mfma_f32_16x16x32_bf16 v[176:179], v[144:147], v[232:235], v[176:179]\n"
    "ds_read_b64_tr_b16 v[144:145], v28 offset:53248\n"
    "ds_read_b64_tr_b16 v[146:147], v28 offset:55296\n"
    "v_mfma_f32_16x16x32_bf16 v[180:183], v[148:151], v[232:235], v[180:183]\n"
    "ds_read_b64_tr_b16 v[148:149], v29 offset:53248\n"
    "ds_read_b64_tr_b16 v[150:151], v29 offset:55296\n"
    "s_waitcnt lgkmcnt(15)\n"
    "v_mfma_f32_16x16x32_bf16 v[184:187], v[152:155], v[232:235], v[184:187]\n"
    "ds_read_b64_tr_b16 v[152:153], v30 offset:53248\n"
    "ds_read_b64_tr_b16 v[154:155], v30 offset:55296\n"
    "v_mfma_f32_16x16x32_bf16 v[188:191], v[156:159], v[232:235], v[188:191]\n"
    "ds_read_b64_tr_b16 v[156:157], v31 offset:53248\n"
    "ds_read_b64_tr_b16 v[158:159], v31 offset:55296\n"
    "s_waitcnt vmcnt(11)\n"
    "s_waitcnt lgkmcnt(15)\n"
    "v_mfma_f32_16x16x32_bf16 v[176:179], v[160:163], v[236:239], v[176:179]\n"
    "ds_read_b64_tr_b16 v[160:161], v28 offset:57344\n"
    "ds_read_b64_tr_b16 v[162:163], v28 offset:59392\n"
    "v_mfma_f32_16x16x32_bf16 v[180:183], v[164:167], v[236:239], v[180:183]\n"
    "ds_read_b64_tr_b16 v[164:165], v29 offset:57344\n"
    "ds_read_b64_tr_b16 v[166:167], v29 offset:59392\n"
    "s_waitcnt lgkmcnt(15)\n"
    "v_mfma_f32_16x16x32_bf16 v[184:187], v[168:171], v[236:239], v[184:187]\n"
    "ds_read_b64_tr_b16 v[168:169], v30 offset:57344\n"
    "ds_read_b64_tr_b16 v[170:171], v30 offset:59392\n"
    "v_mfma_f32_16x16x32_bf16 v[188:191], v[172:175], v[236:239], v[188:191]\n"
    "ds_read_b64_tr_b16 v[172:173], v31 offset:57344\n"
    "ds_read_b64_tr_b16 v[174:175], v31 offset:59392\n"
    "s_waitcnt vmcnt(10)\n"
    "s_waitcnt lgkmcnt(15)\n"
    "v_mfma_f32_16x16x32_bf16 v[176:179], v[128:131], v[240:243], v[176:179]\n"
    "ds_read_b64_tr_b16 v[128:129], v28 offset:61440\n"
    "ds_read_b64_tr_b16 v[130:131], v28 offset:63488\n"
    "v_mfma_f32_16x16x32_bf16 v[180:183], v[132:135], v[240:243], v[180:183]\n"
    "ds_read_b64_tr_b16 v[132:133], v29 offset:61440\n"
    "ds_read_b64_tr_b16 v[134:135], v29 offset:63488\n"
    "s_waitcnt lgkmcnt(15)\n"
    "v_mfma_f32_16x16x32_bf16 v[184:187], v[136:139], v[240:243], v[184:187]\n"
    "ds_read_b64_tr_b16 v[136:137], v30 offset:61440\n"
    "ds_read_b64_tr_b16 v[138:139], v30 offset:63488\n"
    "v_mfma_f32_16x16x32_bf16 v[188:191], v[140:143], v[240:243], v[188:191]\n"
    "ds_read_b64_tr_b16 v[140:141], v31 offset:61440\n"
    "ds_read_b64_tr_b16 v[142:143], v31 offset:63488\n"
    "s_waitcnt vmcnt(9)\n"
    "ds_inc_u32 v2, v2\n"
    "s_waitcnt lgkmcnt(15)\n"
    "v_mfma_f32_16x16x32_bf16 v[176:179], v[144:147], v[244:247], v[176:179]\n"
    "v_mfma_f32_16x16x32_bf16 v[180:183], v[148:151], v[244:247], v[180:183]\n"
    "s_waitcnt lgkmcnt(15)\n"
    "v_mfma_f32_16x16x32_bf16 v[184:187], v[152:155], v[244:247], v[184:187]\n"
    "v_mfma_f32_16x16x32_bf16 v[188:191], v[156:159], v[244:247], v[188:191]\n"
    "s_waitcnt vmcnt(8)\n"
    "s_waitcnt lgkmcnt(13)\n"
    "v_mfma_f32_16x16x32_bf16 v[176:179], v[160:163], v[248:251], v[176:179]\n"
    "v_mfma_f32_16x16x32_bf16 v[180:183], v[164:167], v[248:251], v[180:183]\n"
    "s_waitcnt lgkmcnt(9)\n"
    "v_mfma_f32_16x16x32_bf16 v[184:187], v[168:171], v[248:251], v[184:187]\n"
    "v_mfma_f32_16x16x32_bf16 v[188:191], v[172:175], v[248:251], v[188:191]\n"
    "s_waitcnt vmcnt(7)\n"
    "s_waitcnt lgkmcnt(5)\n"
    "v_mfma_f32_16x16x32_bf16 v[176:179], v[128:131], v[252:255], v[176:179]\n"
    "v_mfma_f32_16x16x32_bf16 v[180:183], v[132:135], v[252:255], v[180:183]\n"
    "s_waitcnt lgkmcnt(1)\n"
    "v_mfma_f32_16x16x32_bf16 v[184:187], v[136:139], v[252:255], v[184:187]\n"
    "v_mfma_f32_16x16x32_bf16 v[188:191], v[140:143], v[252:255], v[188:191]\n"
    "s_nop 3\n"
    "v_cvt_pk_bf16_f32 v192, v176, v177\n"
    "v_cvt_pk_bf16_f32 v193, v178, v179\n"
    "v_cvt_pk_bf16_f32 v194, v180, v181\n"
    "v_cvt_pk_bf16_f32 v195, v182, v183\n"
    "v_cvt_pk_bf16_f32 v196, v184, v185\n"
    "v_cvt_pk_bf16_f32 v197, v186, v187\n"
    "v_cvt_pk_bf16_f32 v198, v188, v189\n"
    "v_cvt_pk_bf16_f32 v199, v190, v191\n"
    "buffer_store_dwordx4 v[192:195], v0, s[24:27], s70 offen sc1\n"
    "buffer_store_dwordx4 v[196:199], v0, s[24:27], s70 offen offset:1024 sc1\n"
    "v_mfma_f32_16x16x32_bf16 v[204:207], v[192:195], v[192:195], 0\n"
    "v_mfma_f32_16x16x32_bf16 v[200:203], v[60:63], v[192:195], 0\n"
    "v_mfma_f32_16x16x32_bf16 v[204:207], v[196:199], v[196:199], v[204:207]\n"
    "v_mfma_f32_16x16x32_bf16 v[200:203], v[60:63], v[196:199], v[200:203]\n"
    "s_mov_b32 s50, 0x200002\n"
    "s_mov_b32 s51, 0x20000200\n"
    "s_mov_b32 s52, 0x400004\n"
    "s_mov_b32 s53, 0x40000400\n"
    "s_mov_b32 s54, 0x800008\n"
    "s_mov_b32 s55, 0x80000800\n"
    "ds_read_b32 v3, v1 offset:0\n"
    "v_cndmask_b32 v204, v204, v205, s[50:51]\n"
    "v_cndmask_b32 v204, v204, v206, s[52:53]\n"
    "v_cndmask_b32 v200, v204, v207, s[54:55]\n"
    "s_mov_b32 exec_lo, 0x00f0000f\n"
    "s_mov_b32 exec_hi, 0xf0000f00\n"
    "buffer_store_dwordx2 v[200:201], v26, s[24:27], s71 offen sc1\n"
    "s_mov_b64 exec, -1\n"
    "s_waitcnt lgkmcnt(0)\n"
    "v_cmp_gt_u32 vcc, 12, v3\n"
    "s_cbranch_vccnz .Ls4_cs28\n"
    ".Ls4_cb29:\n"
    "s_xor_b32 s89, s84, 10\n"
    "s_mul_i32 s89, s89, 88064\n"
    "s_add_u32 m0, s83, 69632\n"
    "s_add_u32 s89, s89, s82\n"
    "buffer_load_dwordx4 v16, s[32:35], s89 offen offset:0 lds\n"
    "buffer_load_dwordx4 v17, s[32:35], s89 offen offset:1024 lds\n"
    "buffer_load_dwordx4 v18, s[32:35], s89 offen offset:2048 lds\n"
    "s_waitcnt vmcnt(6)\n"
    "ds_inc_u32 v2, v2\n"
    "ds_read_b32 v3, v1 offset:0\n"
    "s_waitcnt lgkmcnt(0)\n"
    "v_cmp_gt_u32 vcc, 13, v3\n"
    "s_cbranch_vccnz .Ls4_cs30\n"
    ".Ls4_cb31:\n"
    "ds_read_b64_tr_b16 v[124:125], v19 offset:0\n"
    "ds_read_b64_tr_b16 v[126:127], v19 offset:6144\n"
    "ds_read_b64_tr_b16 v[128:129], v20 offset:0\n"
    "ds_read_b64_tr_b16 v[130:131], v20 offset:6144\n"
    "ds_read_b64_tr_b16 v[132:133], v21 offset:0\n"
    "ds_read_b64_tr_b16 v[134:135], v21 offset:6144\n"
    "ds_read_b64_tr_b16 v[136:137], v19 offset:12288\n"
    "ds_read_b64_tr_b16 v[138:139], v19 offset:18432\n"
    "ds_read_b64_tr_b16 v[140:141], v20 offset:12288\n"
    "ds_read_b64_tr_b16 v[142:143], v20 offset:18432\n"
    "ds_read_b64_tr_b16 v[144:145], v21 offset:12288\n"
    "ds_read_b64_tr_b16 v[146:147], v21 offset:18432\n"
    "ds_read_b64_tr_b16 v[148:149], v19 offset:24576\n"
    "ds_read_b64_tr_b16 v[150:151], v19 offset:30720\n"
    "ds_read_b64_tr_b16 v[152:153], v20 offset:24576\n"
    "ds_read_b64_tr_b16 v[154:155], v20 offset:30720\n"
    "ds_read_b64_tr_b16 v[156:157], v21 offset:24576\n"
    "ds_read_b64_tr_b16 v[158:159], v21 offset:30720\n"
    "ds_read_b64_tr_b16 v[160:161], v19 offset:36864\n"
    "ds_read_b64_tr_b16 v[162:163], v19 offset:43008\n"
    "ds_read_b64_tr_b16 v[164:165], v20 offset:36864\n"
    "ds_read_b64_tr_b16 v[166:167], v20 offset:43008\n"
    "ds_read_b64_tr_b16 v[168:169], v21 offset:36864\n"
    "ds_read_b64_tr_b16 v[170:171], v21 offset:43008\n"
    "ds_read_b64_tr_b16 v[172:173], v19 offset:49152\n"
    "ds_read_b64_tr_b16 v[174:175], v19 offset:55296\n"
    "ds_read_b64_tr_b16 v[176:177], v20 offset:49152\n"
    "ds_read_b64_tr_b16 v[178:179], v20 offset:55296\n"
    "ds_read_b64_tr_b16 v[180:181], v21 offset:49152\n"
    "ds_read_b64_tr_b16 v[182:183], v21 offset:55296\n"
    "ds_inc_u32 v2, v2\n"
    "ds_read_b32 v3, v1 offset:0\n"
    "s_mul_i32 s92, s13, 180224\n"
    "s_add_u32 s92, s92, 9437184\n"
    "s_lshr_b32 s90, s36, 7\n"
    "s_sub_u32 s90, 3, s90\n"
    "s_and_b32 s90, s90, 7\n"
    "s_cmp_eq_u32 s14, 7\n"
    "s_cselect_b32 s50, s90, s14\n"
    "s_cmp_eq_u32 s14, s90\n"
    "s_cselect_b32 s50, 7, s50\n"
    "s_mul_i32 s90, s50, 6\n"
    "s_mul_i32 s91, s15, 3\n"
    "s_add_u32 s91, s91, 1\n"
    "s_lshr_b32 s91, s91, 1\n"
    "s_add_u32 s91, s91, s90\n"
    "s_lshl_b32 s48, s91, 10\n"
    "s_add_u32 s48, s48, s92\n"
    "s_cmp_lt_u32 s91, 43\n"
    "s_cselect_b32 s48, s48, 0x40000000\n"
    "s_lshr_b32 s91, s15, 1\n"
    "s_mul_i32 s91, s91, 3\n"
    "s_add_u32 s91, s91, s90\n"
    "s_add_u32 s91, s91, 1\n"
    "s_lshl_b32 s49, s91, 10\n"
    "s_add_u32 s49, s49, s92\n"
    "s_and_b32 s90, s15, 1\n"
    "s_lshl_b32 s90, s90, 9\n"
    "s_add_u32 s49, s90, s49\n"
    "s_cmp_lt_u32 s91, 43\n"
    "s_cselect_b32 s49, s49, 0x40000000\n"
    "s_lshl_b32 s90, s50, 7\n"
    "s_lshl_b32 s50, s13, 12\n"
    "s_add_u32 s50, s90, s50\n"
    "s_add_u32 s50, s50, 327680\n"
    "v_add_u32 v48, 69632, v19\n"
    "v_add_u32 v49, 69632, v20\n"
    "v_add_u32 v50, 69632, v21\n"
    "s_mov_b64 exec, 0xff\n"
    "s_waitcnt vmcnt(3)\n"
    "ds_inc_rtn_u32 v6, v11, v2 offset:32\n"
    "s_mov_b64 exec, -1\n"
    "s_mov_b64 exec, 0xff\n"
    "s_waitcnt lgkmcnt(0)\n"
    "v_cmpx_eq_u32 vcc, 3, v6\n"
    "buffer_store_dwordx4 v[4:7], v0, s[24:27], s73 offen sc1\n"
    "s_mov_b64 exec, -1\n"
    "s_xor_b32 s89, s84, 11\n"
    "s_mul_i32 s89, s89, 88064\n"
    "s_add_u32 m0, s83, 81920\n"
    "s_add_u32 s89, s89, s82\n"
    "buffer_load_dwordx4 v16, s[32:35], s89 offen offset:0 lds\n"
    "buffer_load_dwordx4 v17, s[32:35], s89 offen offset:1024 lds\n"
    "buffer_load_dwordx4 v18, s[32:35], s89 offen offset:2048 lds\n"
    "s_xor_b32 s89, s84, 12\n"
    "s_mul_i32 s89, s89, 88064\n"
    "s_add_u32 m0, s83, 94208\n"
    "s_add_u32 s89, s89, s82\n"
    "buffer_load_dwordx4 v16, s[32:35], s89 offen offset:0 lds\n"
    "buffer_load_dwordx4 v17, s[32:35], s89 offen offset:1024 lds\n"
    "buffer_load_dwordx4 v18, s[32:35], s89 offen offset:2048 lds\n"
    "s_xor_b32 s89, s84, 13\n"
    "s_mul_i32 s89, s89, 88064\n"
    "s_add_u32 m0, s83, 106496\n"
    "s_add_u32 s89, s89, s82\n"
    "buffer_load_dwordx4 v16, s[32:35], s89 offen offset:0 lds\n"
    "buffer_load_dwordx4 v17, s[32:35], s89 offen offset:1024 lds\n"
    "buffer_load_dwordx4 v18, s[32:35], s89 offen offset:2048 lds\n"
    "s_xor_b32 s89, s84, 14\n"
    "s_mul_i32 s89, s89, 88064\n"
    "s_add_u32 m0, s83, 118784\n"
    "s_add_u32 s89, s89, s82\n"
    "buffer_load_dwordx4 v16, s[32:35], s89 offen offset:0 lds\n"
    "buffer_load_dwordx4 v17, s[32:35], s89 offen offset:1024 lds\n"
    "buffer_load_dwordx4 v18, s[32:35], s89 offen offset:2048 lds\n"
    "v_cmp_gt_u32 vcc, 14, v3\n"
    "s_cbranch_vccnz .Ls4_cs32\n"
    ".Ls4_cb33:\n"
    "s_xor_b32 s89, s84, 15\n"
    "s_mul_i32 s89, s89, 88064\n"
    "s_add_u32 m0, s83, 49152\n"
    "s_add_u32 s89, s89, s82\n"
    "buffer_load_dwordx4 v16, s[32:35], s89 offen offset:0 lds\n"
    "buffer_load_dwordx4 v17, s[32:35], s89 offen offset:1024 lds\n"
    "buffer_load_dwordx4 v18, s[32:35], s89 offen offset:2048 lds\n"
    "buffer_load_dwordx2 v[6:7], v24, s[24:27], s75 offen sc1\n"
    "v_mfma_f32_16x16x32_bf16 v[32:35], v[124:127], v[60:63], v[32:35]\n"
    "v_mfma_f32_16x16x32_bf16 v[36:39], v[128:131], v[60:63], v[36:39]\n"
    "v_mfma_f32_16x16x32_bf16 v[40:43], v[132:135], v[60:63], v[40:43]\n"
    "v_mfma_f32_16x16x32_bf16 v[32:35], v[136:139], v[60:63], v[32:35]\n"
    "v_mfma_f32_16x16x32_bf16 v[36:39], v[140:143], v[60:63], v[36:39]\n"
    "v_mfma_f32_16x16x32_bf16 v[40:43], v[144:147], v[60:63], v[40:43]\n"
    "v_mfma_f32_16x16x32_bf16 v[32:35], v[148:151], v[60:63], v[32:35]\n"
    "v_mfma_f32_16x16x32_bf16 v[36:39], v[152:155], v[60:63], v[36:39]\n"
    "v_mfma_f32_16x16x32_bf16 v[40:43], v[156:159], v[60:63], v[40:43]\n"
    "v_mfma_f32_16x16x32_bf16 v[32:35], v[160:163], v[60:63], v[32:35]\n"
    "v_mfma_f32_16x16x32_bf16 v[36:39], v[164:167], v[60:63], v[36:39]\n"
    "v_mfma_f32_16x16x32_bf16 v[40:43], v[168:171], v[60:63], v[40:43]\n"
    "v_mfma_f32_16x16x32_bf16 v[32:35], v[172:175], v[60:63], v[32:35]\n"
    "v_mfma_f32_16x16x32_bf16 v[36:39], v[176:179], v[60:63], v[36:39]\n"
    "v_mfma_f32_16x16x32_bf16 v[40:43], v[180:183], v[60:63], v[40:43]\n"
    "s_waitcnt vmcnt(0)\n"
    "v_cmp_ne_u64 vcc, s[96:97], v[6:7]\n"
    "s_cbranch_vccnz .Ls4_ps34\n"
    ".Ls4_pb35:\n"
    "s_add_u32 m0, s87, 0\n"
    "s_nop 0\n"
    "buffer_load_dwordx4 v0, s[24:27], s85 offen offset:0 sc1 lds\n"
    "buffer_load_dwordx4 v0, s[24:27], s85 offen offset:1024 sc1 lds\n"
    "buffer_load_dwordx4 v0, s[24:27], s86 offen offset:2048 sc1 lds\n"
    "buffer_load_dwordx4 v0, s[24:27], s86 offen offset:3072 sc1 lds\n"
    "buffer_load_dwordx2 v[44:45], v25, s[24:27], s64 offen sc1\n"
    "buffer_load_dwordx2 v[46:47], v25, s[24:27], s64 offen offset:128 sc1\n"
    "s_add_u32 m0, s87, 16384\n"
    "s_add_u32 s89, s85, 16384\n"
    "s_add_u32 s90, s86, 16384\n"
    "buffer_load_dwordx4 v0, s[24:27], s89 offen offset:0 sc1 lds\n"
    "buffer_load_dwordx4 v0, s[24:27], s89 offen offset:1024 sc1 lds\n"
    "buffer_load_dwordx4 v0, s[24:27], s90 offen offset:2048 sc1 lds\n"
    "buffer_load_dwordx4 v0, s[24:27], s90 offen offset:3072 sc1 lds\n"
    "s_add_u32 m0, s87, 32768\n"
    "s_add_u32 s89, s85, 32768\n"
    "s_add_u32 s90, s86, 32768\n"
    "buffer_load_dwordx4 v0, s[24:27], s89 offen offset:0 sc1 lds\n"
    "buffer_load_dwordx4 v0, s[24:27], s89 offen offset:1024 sc1 lds\n"
    "buffer_load_dwordx4 v0, s[24:27], s90 offen offset:2048 sc1 lds\n"
    "buffer_load_dwordx4 v0, s[24:27], s90 offen offset:3072 sc1 lds\n"
    "ds_inc_u32 v2, v2\n"
    "ds_read_b32 v3, v1 offset:0\n"
    "s_waitcnt lgkmcnt(0)\n"
    "v_cmp_gt_u32 vcc, 15, v3\n"
    "s_cbranch_vccnz .Ls4_cs36\n"
    ".Ls4_cb37:\n"
    "ds_read_b64_tr_b16 v[184:185], v48 offset:0\n"
    "ds_read_b64_tr_b16 v[186:187], v48 offset:6144\n"
    "ds_read_b64_tr_b16 v[188:189], v49 offset:0\n"
    "ds_read_b64_tr_b16 v[190:191], v49 offset:6144\n"
    "ds_read_b64_tr_b16 v[192:193], v50 offset:0\n"
    "ds_read_b64_tr_b16 v[194:195], v50 offset:6144\n"
    "ds_read_b64_tr_b16 v[196:197], v48 offset:12288\n"
    "ds_read_b64_tr_b16 v[198:199], v48 offset:18432\n"
    "ds_read_b64_tr_b16 v[200:201], v49 offset:12288\n"
    "ds_read_b64_tr_b16 v[202:203], v49 offset:18432\n"
    "ds_read_b64_tr_b16 v[204:205], v50 offset:12288\n"
    "ds_read_b64_tr_b16 v[206:207], v50 offset:18432\n"
    "ds_read_b64_tr_b16 v[208:209], v48 offset:24576\n"
    "ds_read_b64_tr_b16 v[210:211], v48 offset:30720\n"
    "ds_read_b64_tr_b16 v[212:213], v49 offset:24576\n"
    "ds_read_b64_tr_b16 v[214:215], v49 offset:30720\n"
    "ds_read_b64_tr_b16 v[216:217], v50 offset:24576\n"
    "ds_read_b64_tr_b16 v[218:219], v50 offset:30720\n"
    "ds_read_b64_tr_b16 v[220:221], v48 offset:36864\n"
    "ds_read_b64_tr_b16 v[222:223], v48 offset:43008\n"
    "ds_read_b64_tr_b16 v[224:225], v49 offset:36864\n"
    "ds_read_b64_tr_b16 v[226:227], v49 offset:43008\n"
    "ds_read_b64_tr_b16 v[228:229], v50 offset:36864\n"
    "ds_read_b64_tr_b16 v[230:231], v50 offset:43008\n"
    "ds_read_b64_tr_b16 v[232:233], v48 offset:49152\n"
    "ds_read_b64_tr_b16 v[234:235], v48 offset:55296\n"
    "ds_read_b64_tr_b16 v[236:237], v49 offset:49152\n"
    "ds_read_b64_tr_b16 v[238:239], v49 offset:55296\n"
    "ds_read_b64_tr_b16 v[240:241], v50 offset:49152\n"
    "ds_read_b64_tr_b16 v[242:243], v50 offset:55296\n"
    "ds_read_b64_tr_b16 v[244:245], v19 offset:49152\n"
    "ds_read_b64_tr_b16 v[246:247], v19 offset:55296\n"
    "ds_read_b64_tr_b16 v[248:249], v20 offset:49152\n"
    "ds_read_b64_tr_b16 v[250:251], v20 offset:55296\n"
    "ds_read_b64_tr_b16 v[252:253], v21 offset:49152\n"
    "ds_read_b64_tr_b16 v[254:255], v21 offset:55296\n"
    "ds_inc_u32 v2, v2\n"
    "ds_read_b32 v3, v1 offset:0\n"
    "s_waitcnt lgkmcnt(15)\n"
    "v_mfma_f32_16x16x32_bf16 v[32:35], v[184:187], v[60:63], v[32:35]\n"
    "v_mfma_f32_16x16x32_bf16 v[36:39], v[188:191], v[60:63], v[36:39]\n"
    "v_mfma_f32_16x16x32_bf16 v[40:43], v[192:195], v[60:63], v[40:43]\n"
    "s_waitcnt lgkmcnt(15)\n"
    "v_mfma_f32_16x16x32_bf16 v[32:35], v[196:199], v[60:63], v[32:35]\n"
    "v_mfma_f32_16x16x32_bf16 v[36:39], v[200:203], v[60:63], v[36:39]\n"
    "v_mfma_f32_16x16x32_bf16 v[40:43], v[204:207], v[60:63], v[40:43]\n"
    "s_waitcnt lgkmcnt(15)\n"
    "v_mfma_f32_16x16x32_bf16 v[32:35], v[208:211], v[60:63], v[32:35]\n"
    "v_mfma_f32_16x16x32_bf16 v[36:39], v[212:215], v[60:63], v[36:39]\n"
    "v_mfma_f32_16x16x32_bf16 v[40:43], v[216:219], v[60:63], v[40:43]\n"
    "s_waitcnt lgkmcnt(14)\n"
    "v_mfma_f32_16x16x32_bf16 v[32:35], v[220:223], v[60:63], v[32:35]\n"
    "v_mfma_f32_16x16x32_bf16 v[36:39], v[224:227], v[60:63], v[36:39]\n"
    "v_mfma_f32_16x16x32_bf16 v[40:43], v[228:231], v[60:63], v[40:43]\n"
    "s_waitcnt lgkmcnt(8)\n"
    "v_mfma_f32_16x16x32_bf16 v[32:35], v[232:235], v[60:63], v[32:35]\n"
    "v_mfma_f32_16x16x32_bf16 v[36:39], v[236:239], v[60:63], v[36:39]\n"
    "v_mfma_f32_16x16x32_bf16 v[40:43], v[240:243], v[60:63], v[40:43]\n"
    "s_waitcnt lgkmcnt(2)\n"
    "v_mfma_f32_16x16x32_bf16 v[32:35], v[244:247], v[60:63], v[32:35]\n"
    "v_mfma_f32_16x16x32_bf16 v[36:39], v[248:251], v[60:63], v[36:39]\n"
    "v_mfma_f32_16x16x32_bf16 v[40:43], v[252:255], v[60:63], v[40:43]\n"
    "s_waitcnt lgkmcnt(0)\n"
    "v_cmp_gt_u32 vcc, 16, v3\n"
    "s_cbranch_vccnz .Ls4_cs38\n"
    ".Ls4_cb39:\n"
    "s_add_u32 m0, s87, 49152\n"
    "s_add_u32 s89, s85, 49152\n"
    "s_add_u32 s90, s86, 49152\n"
    "buffer_load_dwordx4 v0, s[24:27], s89 offen offset:0 sc1 lds\n"
    "buffer_load_dwordx4 v0, s[24:27], s89 offen offset:1024 sc1 lds\n"
    "buffer_load_dwordx4 v0, s[24:27], s90 offen offset:2048 sc1 lds\n"
    "buffer_load_dwordx4 v0, s[24:27], s90 offen offset:3072 sc1 lds\n"
    "s_waitcnt vmcnt(12)\n"
    "v_add_f32 v44, v44, v46\n"
    "v_add_f32 v45, v45, v47\n"
    "s_nop 0\n"
    "v_mfma_f32_16x16x4_f32 v[48:51], v10, v44, 0\n"
    "v_mfma_f32_16x16x4_f32 v[52:55], v10, v45, 0\n"
    "s_nop 9\n"
    "v_mul_f32 v52, 0x3b000000, v52\n"
    "v_mul_f32 v48, 0x3b000000, v48\n"
    "v_fma_f32 v48, -v52, v52, v48\n"
    "v_cvt_pk_bf16_f32 v49, v52, v52\n"
    "v_max_f32 v48, 0, v48\n"
    "v_and_b32 v49, 0xffff0000, v49\n"
    "v_cvt_pk_bf16_f32 v50, v48, v48\n"
    "v_and_b32 v50, 0xffff0000, v50\n"
    "v_add_f32 v50, 0x3727c5ac, v50\n"
    "v_cvt_pk_bf16_f32 v50, v50, v50\n"
    "v_and_b32 v50, 0xffff0000, v50\n"
    "v_sqrt_f32 v50, v50\n"
    "s_nop 0\n"
    "v_cvt_pk_bf16_f32 v45, v50, v50\n"
    "v_and_b32 v45, 0xffff0000, v45\n"
    "v_rcp_f32 v50, v45\n"
    "v_sub_f32 v44, 0, v49\n"
    "v_mul_f32 v47, 0xc0135761, v50\n"
    "v_mul_f32 v50, v50, v50\n"
    "v_mul_f32 v50, 0x3d372713, v50\n"
    "v_mul_f32 v46, v47, v50\n"
    "ds_write_b128 v27, v[44:47]\n"
    "ds_inc_u32 v2, v2\n"
    "ds_read_b32 v3, v1 offset:0\n"
    "s_waitcnt lgkmcnt(0)\n"
    "v_cmp_gt_u32 vcc, 17, v3\n"
    "s_cbranch_vccnz .Ls4_cs40\n"
    ".Ls4_cb41:\n"
    "ds_read_b128 v[16:19], v0 offset:0\n"
    "ds_read_b128 v[20:23], v0 offset:1024\n"
    "v_lshrrev_b32 v3, 2, v0\n"
    "v_add_u32 v3, 160064, v3\n"
    "ds_read_b128 v[24:27], v0 offset:2048\n"
    "s_waitcnt lgkmcnt(2)\n"
    "v_mfma_f32_16x16x32_bf16 v[44:47], v[64:67], v[16:19], 0\n"
    "v_mfma_f32_16x16x32_bf16 v[48:51], v[68:71], v[16:19], 0\n"
    "v_mfma_f32_16x16x32_bf16 v[52:55], v[72:75], v[16:19], 0\n"
    "ds_read_b128 v[16:19], v0 offset:3072\n"
    "s_waitcnt lgkmcnt(2)\n"
    "v_mfma_f32_16x16x32_bf16 v[44:47], v[76:79], v[20:23], v[44:47]\n"
    "v_mfma_f32_16x16x32_bf16 v[48:51], v[80:83], v[20:23], v[48:51]\n"
    "v_mfma_f32_16x16x32_bf16 v[52:55], v[84:87], v[20:23], v[52:55]\n"
    "ds_read_b128 v[20:23], v0 offset:4096\n"
    "s_waitcnt lgkmcnt(2)\n"
    "v_mfma_f32_16x16x32_bf16 v[44:47], v[88:91], v[24:27], v[44:47]\n"
    "v_mfma_f32_16x16x32_bf16 v[48:51], v[92:95], v[24:27], v[48:51]\n"
    "v_mfma_f32_16x16x32_bf16 v[52:55], v[96:99], v[24:27], v[52:55]\n"
    "ds_read_b128 v[24:27], v0 offset:5120\n"
    "s_waitcnt lgkmcnt(2)\n"
    "v_mfma_f32_16x16x32_bf16 v[44:47], v[100:103], v[16:19], v[44:47]\n"
    "v_mfma_f32_16x16x32_bf16 v[48:51], v[104:107], v[16:19], v[48:51]\n"
    "v_mfma_f32_16x16x32_bf16 v[52:55], v[108:111], v[16:19], v[52:55]\n"
    "ds_read_b128 v[16:19], v0 offset:6144\n"
    "s_waitcnt lgkmcnt(2)\n"
    "v_mfma_f32_16x16x32_bf16 v[44:47], v[112:115], v[20:23], v[44:47]\n"
    "v_mfma_f32_16x16x32_bf16 v[48:51], v[116:119], v[20:23], v[48:51]\n"
    "v_mfma_f32_16x16x32_bf16 v[52:55], v[120:123], v[20:23], v[52:55]\n"
    "ds_read_b128 v[20:23], v0 offset:7168\n"
    "s_waitcnt lgkmcnt(2)\n"
    "v_mfma_f32_16x16x32_bf16 v[44:47], v[124:127], v[24:27], v[44:47]\n"
    "v_mfma_f32_16x16x32_bf16 v[48:51], v[128:131], v[24:27], v[48:51]\n"
    "v_mfma_f32_16x16x32_bf16 v[52:55], v[132:135], v[24:27], v[52:55]\n"
    "ds_read_b128 v[24:27], v0 offset:8192\n"
    "s_waitcnt lgkmcnt(2)\n"
    "v_mfma_f32_16x16x32_bf16 v[44:47], v[136:139], v[16:19], v[44:47]\n"
    "v_mfma_f32_16x16x32_bf16 v[48:51], v[140:143], v[16:19], v[48:51]\n"
    "v_mfma_f32_16x16x32_bf16 v[52:55], v[144:147], v[16:19], v[52:55]\n"
    "ds_read_b128 v[16:19], v0 offset:9216\n"
    "s_waitcnt lgkmcnt(2)\n"
    "v_mfma_f32_16x16x32_bf16 v[44:47], v[148:151], v[20:23], v[44:47]\n"
    "v_mfma_f32_16x16x32_bf16 v[48:51], v[152:155], v[20:23], v[48:51]\n"
    "v_mfma_f32_16x16x32_bf16 v[52:55], v[156:159], v[20:23], v[52:55]\n"
    "ds_read_b128 v[20:23], v0 offset:10240\n"
    "s_waitcnt lgkmcnt(2)\n"
    "v_mfma_f32_16x16x32_bf16 v[44:47], v[160:163], v[24:27], v[44:47]\n"
    "v_mfma_f32_16x16x32_bf16 v[48:51], v[164:167], v[24:27], v[48:51]\n"
    "v_mfma_f32_16x16x32_bf16 v[52:55], v[168:171], v[24:27], v[52:55]\n"
    "ds_read_b128 v[24:27], v0 offset:11264\n"
    "s_waitcnt lgkmcnt(2)\n"
    "v_mfma_f32_16x16x32_bf16 v[44:47], v[172:175], v[16:19], v[44:47]\n"
    "v_mfma_f32_16x16x32_bf16 v[48:51], v[176:179], v[16:19], v[48:51]\n"
    "v_mfma_f32_16x16x32_bf16 v[52:55], v[180:183], v[16:19], v[52:55]\n"
    "s_waitcnt vmcnt(8)\n"
    "ds_inc_u32 v2, v2\n"
    "ds_read_b128 v[16:19], v0 offset:12288\n"
    "s_waitcnt lgkmcnt(3)\n"
    "v_mfma_f32_16x16x32_bf16 v[44:47], v[184:187], v[20:23], v[44:47]\n"
    "v_mfma_f32_16x16x32_bf16 v[48:51], v[188:191], v[20:23], v[48:51]\n"
    "v_mfma_f32_16x16x32_bf16 v[52:55], v[192:195], v[20:23], v[52:55]\n"
    "ds_read_b128 v[20:23], v0 offset:13312\n"
    "s_waitcnt lgkmcnt(3)\n"
    "v_mfma_f32_16x16x32_bf16 v[44:47], v[196:199], v[24:27], v[44:47]\n"
    "v_mfma_f32_16x16x32_bf16 v[48:51], v[200:203], v[24:27], v[48:51]\n"
    "v_mfma_f32_16x16x32_bf16 v[52:55], v[204:207], v[24:27], v[52:55]\n"
    "ds_read_b128 v[24:27], v0 offset:14336\n"
    "s_waitcnt lgkmcnt(2)\n"
    "v_mfma_f32_16x16x32_bf16 v[44:47], v[208:211], v[16:19], v[44:47]\n"
    "v_mfma_f32_16x16x32_bf16 v[48:51], v[212:215], v[16:19], v[48:51]\n"
    "v_mfma_f32_16x16x32_bf16 v[52:55], v[216:219], v[16:19], v[52:55]\n"
    "ds_read_b32 v9, v1 offset:0\n"
    "ds_read_b128 v[16:19], v0 offset:15360\n"
    "s_waitcnt lgkmcnt(3)\n"
    "v_mfma_f32_16x16x32_bf16 v[44:47], v[220:223], v[20:23], v[44:47]\n"
    "v_mfma_f32_16x16x32_bf16 v[48:51], v[224:227], v[20:23], v[48:51]\n"
    "v_mfma_f32_16x16x32_bf16 v[52:55], v[228:231], v[20:23], v[52:55]\n"
    "s_waitcnt lgkmcnt(1)\n"
    "v_cmp_gt_u32 vcc, 18, v9\n"
    "s_cbranch_vccnz .Ls4_cs42\n"
    ".Ls4_cb43:\n"
    "ds_read_b128 v[20:23], v0 offset:16384\n"
    "v_mfma_f32_16x16x32_bf16 v[44:47], v[232:235], v[24:27], v[44:47]\n"
    "v_mfma_f32_16x16x32_bf16 v[48:51], v[236:239], v[24:27], v[48:51]\n"
    "v_mfma_f32_16x16x32_bf16 v[52:55], v[240:243], v[24:27], v[52:55]\n"
    "ds_read_b128 v[24:27], v0 offset:17408\n"
    "s_waitcnt lgkmcnt(2)\n"
    "v_mfma_f32_16x16x32_bf16 v[44:47], v[244:247], v[16:19], v[44:47]\n"
    "v_mfma_f32_16x16x32_bf16 v[48:51], v[248:251], v[16:19], v[48:51]\n"
    "v_mfma_f32_16x16x32_bf16 v[52:55], v[252:255], v[16:19], v[52:55]\n"
    "v_add_u32 v12, 65536, v0\n"
    "ds_read_b128 v[12:15], v12 offset:0\n"
    "ds_read_b128 v[16:19], v0 offset:18432\n"
    "s_waitcnt lgkmcnt(3)\n"
    "v_mfma_f32_16x16x32_bf16 v[56:59], v[64:67], v[20:23], 0\n"
    "v_mfma_f32_16x16x32_bf16 v[60:63], v[68:71], v[20:23], 0\n"
    "v_mfma_f32_16x16x32_bf16 v[28:31], v[72:75], v[20:23], 0\n"
    "ds_read_b128 v[20:23], v0 offset:19456\n"
    "s_waitcnt lgkmcnt(3)\n"
    "v_mfma_f32_16x16x32_bf16 v[56:59], v[76:79], v[24:27], v[56:59]\n"
    "s_waitcnt lgkmcnt(2)\n"
    "v_fmac_f32 v44, v32, v12\n"
    "v_fmac_f32 v45, v33, v12\n"
    "v_mfma_f32_16x16x32_bf16 v[60:63], v[80:83], v[24:27], v[60:63]\n"
    "v_fmac_f32 v46, v34, v12\n"
    "v_fmac_f32 v47, v35, v12\n"
    "v_mul_f32 v6, v44, v44\n"
    "v_mfma_f32_16x16x32_bf16 v[28:31], v[84:87], v[24:27], v[28:31]\n"
    "v_mul_f32 v7, v45, v45\n"
    "v_mul_f32 v10, v46, v46\n"
    "v_mul_f32 v11, v47, v47\n"
    "ds_read_b128 v[24:27], v0 offset:20480\n"
    "s_waitcnt lgkmcnt(2)\n"
    "v_mfma_f32_16x16x32_bf16 v[56:59], v[88:91], v[16:19], v[56:59]\n"
    "v_fma_f32 v6, v6, v14, v15\n"
    "v_fma_f32 v7, v7, v14, v15\n"
    "v_fma_f32 v10, v10, v14, v15\n"
    "v_mfma_f32_16x16x32_bf16 v[60:63], v[92:95], v[16:19], v[60:63]\n"
    "v_fma_f32 v11, v11, v14, v15\n"
    "v_mul_f32 v6, v6, v44\n"
    "v_mul_f32 v7, v7, v45\n"
    "v_mfma_f32_16x16x32_bf16 v[28:31], v[96:99], v[16:19], v[28:31]\n"
    "v_mul_f32 v10, v10, v46\n"
    "v_mul_f32 v11, v11, v47\n"
    "v_exp_f32 v6, v6\n"
    "ds_read_b128 v[16:19], v0 offset:21504\n"
    "s_waitcnt lgkmcnt(2)\n"
    "v_mfma_f32_16x16x32_bf16 v[56:59], v[100:103], v[20:23], v[56:59]\n"
    "v_exp_f32 v7, v7\n"
    "v_exp_f32 v10, v10\n"
    "v_exp_f32 v11, v11\n"
    "v_mfma_f32_16x16x32_bf16 v[60:63], v[104:107], v[20:23], v[60:63]\n"
    "v_fma_f32 v6, v6, v13, v13\n"
    "v_fma_f32 v7, v7, v13, v13\n"
    "v_mfma_f32_16x16x32_bf16 v[28:31], v[108:111], v[20:23], v[28:31]\n"
    "v_fma_f32 v10, v10, v13, v13\n"
    "v_fma_f32 v11, v11, v13, v13\n"
    "v_rcp_f32 v6, v6\n"
    "ds_read_b128 v[20:23], v0 offset:22528\n"
    "s_waitcnt lgkmcnt(2)\n"
    "v_mfma_f32_16x16x32_bf16 v[56:59], v[112:115], v[24:27], v[56:59]\n"
    "v_rcp_f32 v7, v7\n"
    "v_rcp_f32 v10, v10\n"
    "v_rcp_f32 v11, v11\n"
    "v_mfma_f32_16x16x32_bf16 v[60:63], v[116:119], v[24:27], v[60:63]\n"
    "v_mul_f32 v44, v44, v6\n"
    "v_mul_f32 v45, v45, v7\n"
    "v_mul_f32 v46, v46, v10\n"
    "v_mfma_f32_16x16x32_bf16 v[28:31], v[120:123], v[24:27], v[28:31]\n"
    "v_mul_f32 v47, v47, v11\n"
    "v_cvt_pk_bf16_f32 v44, v44, v45\n"
    "v_cvt_pk_bf16_f32 v45, v46, v47\n"
    "ds_read_b128 v[24:27], v0 offset:23552\n"
    "s_waitcnt lgkmcnt(2)\n"
    "v_mfma_f32_16x16x32_bf16 v[56:59], v[124:127], v[16:19], v[56:59]\n"
    "v_fmac_f32 v48, v36, v12\n"
    "v_fmac_f32 v49, v37, v12\n"
    "v_fmac_f32 v50, v38, v12\n"
    "v_mfma_f32_16x16x32_bf16 v[60:63], v[128:131], v[16:19], v[60:63]\n"
    "v_fmac_f32 v51, v39, v12\n"
    "v_mul_f32 v6, v48, v48\n"
    "v_mul_f32 v7, v49, v49\n"
    "v_mfma_f32_16x16x32_bf16 v[28:31], v[132:135], v[16:19], v[28:31]\n"
    "v_mul_f32 v10, v50, v50\n"
    "v_mul_f32 v11, v51, v51\n"
    "ds_read_b128 v[16:19], v0 offset:24576\n"
    "s_waitcnt lgkmcnt(2)\n"
    "v_mfma_f32_16x16x32_bf16 v[56:59], v[136:139], v[20:23], v[56:59]\n"
    "v_fma_f32 v6, v6, v14, v15\n"
    "v_fma_f32 v7, v7, v14, v15\n"
    "v_fma_f32 v10, v10, v14, v15\n"
    "v_mfma_f32_16x16x32_bf16 v[60:63], v[140:143], v[20:23], v[60:63]\n"
    "v_fma_f32 v11, v11, v14, v15\n"
    "v_mul_f32 v6, v6, v48\n"
    "v_mul_f32 v7, v7, v49\n"
    "v_mfma_f32_16x16x32_bf16 v[28:31], v[144:147], v[20:23], v[28:31]\n"
    "v_mul_f32 v10, v10, v50\n"
    "v_mul_f32 v11, v11, v51\n"
    "v_exp_f32 v6, v6\n"
    "ds_read_b128 v[20:23], v0 offset:25600\n"
    "s_waitcnt lgkmcnt(2)\n"
    "v_mfma_f32_16x16x32_bf16 v[56:59], v[148:151], v[24:27], v[56:59]\n"
    "v_exp_f32 v7, v7\n"
    "v_exp_f32 v10, v10\n"
    "v_exp_f32 v11, v11\n"
    "v_mfma_f32_16x16x32_bf16 v[60:63], v[152:155], v[24:27], v[60:63]\n"
    "v_fma_f32 v6, v6, v13, v13\n"
    "v_fma_f32 v7, v7, v13, v13\n"
    "v_fma_f32 v10, v10, v13, v13\n"
    "v_mfma_f32_16x16x32_bf16 v[28:31], v[156:159], v[24:27], v[28:31]\n"
    "v_fma_f32 v11, v11, v13, v13\n"
    "v_rcp_f32 v6, v6\n"
    "v_rcp_f32 v7, v7\n"
    "ds_read_b128 v[24:27], v0 offset:26624\n"
    "s_waitcnt lgkmcnt(2)\n"
    "v_mfma_f32_16x16x32_bf16 v[56:59], v[160:163], v[16:19], v[56:59]\n"
    "v_rcp_f32 v10, v10\n"
    "v_rcp_f32 v11, v11\n"
    "v_mul_f32 v48, v48, v6\n"
    "v_mfma_f32_16x16x32_bf16 v[60:63], v[164:167], v[16:19], v[60:63]\n"
    "v_mul_f32 v49, v49, v7\n"
    "v_mul_f32 v50, v50, v10\n"
    "v_mfma_f32_16x16x32_bf16 v[28:31], v[168:171], v[16:19], v[28:31]\n"
    "v_mul_f32 v51, v51, v11\n"
    "v_cvt_pk_bf16_f32 v46, v48, v49\n"
    "v_cvt_pk_bf16_f32 v47, v50, v51\n"
    "ds_read_b128 v[16:19], v0 offset:27648\n"
    "s_waitcnt lgkmcnt(2)\n"
    "v_mfma_f32_16x16x32_bf16 v[56:59], v[172:175], v[20:23], v[56:59]\n"
    "buffer_store_dwordx4 v[44:47], v0, s[24:27], s48 offen sc1\n"
    "s_add_u32 s48, s48, 45056\n"
    "v_fmac_f32 v52, v40, v12\n"
    "v_fmac_f32 v53, v41, v12\n"
    "v_mfma_f32_16x16x32_bf16 v[60:63], v[176:179], v[20:23], v[60:63]\n"
    "v_fmac_f32 v54, v42, v12\n"
    "v_fmac_f32 v55, v43, v12\n"
    "v_mul_f32 v6, v52, v52\n"
    "v_mfma_f32_16x16x32_bf16 v[28:31], v[180:183], v[20:23], v[28:31]\n"
    "v_mul_f32 v7, v53, v53\n"
    "v_mul_f32 v10, v54, v54\n"
    "v_mul_f32 v11, v55, v55\n"
    "s_waitcnt vmcnt(5)\n"
    "ds_inc_u32 v2, v2\n"
    "ds_read_b128 v[20:23], v0 offset:28672\n"
    "s_waitcnt lgkmcnt(3)\n"
    "v_mfma_f32_16x16x32_bf16 v[56:59], v[184:187], v[24:27], v[56:59]\n"
    "v_fma_f32 v6, v6, v14, v15\n"
    "v_fma_f32 v7, v7, v14, v15\n"
    "v_fma_f32 v10, v10, v14, v15\n"
    "v_mfma_f32_16x16x32_bf16 v[60:63], v[188:191], v[24:27], v[60:63]\n"
    "v_fma_f32 v11, v11, v14, v15\n"
    "v_mul_f32 v6, v6, v52\n"
    "v_mul_f32 v7, v7, v53\n"
    "v_mfma_f32_16x16x32_bf16 v[28:31], v[192:195], v[24:27], v[28:31]\n"
    "v_mul_f32 v10, v10, v54\n"
    "v_mul_f32 v11, v11, v55\n"
    "ds_read_b128 v[24:27], v0 offset:29696\n"
    "s_waitcnt lgkmcnt(3)\n"
    "v_mfma_f32_16x16x32_bf16 v[56:59], v[196:199], v[16:19], v[56:59]\n"
    "v_exp_f32 v6, v6\n"
    "v_exp_f32 v7, v7\n"
    "v_exp_f32 v10, v10\n"
    "v_mfma_f32_16x16x32_bf16 v[60:63], v[200:203], v[16:19], v[60:63]\n"
    "v_exp_f32 v11, v11\n"
    "v_fma_f32 v6, v6, v13, v13\n"
    "v_fma_f32 v7, v7, v13, v13\n"
    "v_mfma_f32_16x16x32_bf16 v[28:31], v[204:207], v[16:19], v[28:31]\n"
    "v_fma_f32 v10, v10, v13, v13\n"
    "v_fma_f32 v11, v11, v13, v13\n"
    "v_rcp_f32 v6, v6\n"
    "ds_read_b128 v[16:19], v0 offset:30720\n"
    "s_waitcnt lgkmcnt(2)\n"
    "v_mfma_f32_16x16x32_bf16 v[56:59], v[208:211], v[20:23], v[56:59]\n"
    "v_rcp_f32 v7, v7\n"
    "v_rcp_f32 v10, v10\n"
    "v_rcp_f32 v11, v11\n"
    "v_mfma_f32_16x16x32_bf16 v[60:63], v[212:215], v[20:23], v[60:63]\n"
    "v_mul_f32 v52, v52, v6\n"
    "v_mul_f32 v53, v53, v7\n"
    "v_mul_f32 v54, v54, v10\n"
    "v_mfma_f32_16x16x32_bf16 v[28:31], v[216:219], v[20:23], v[28:31]\n"
    "v_mul_f32 v55, v55, v11\n"
    "v_cvt_pk_bf16_f32 v48, v52, v53\n"
    "v_cvt_pk_bf16_f32 v49, v54, v55\n"
    "ds_read_b32 v9, v1 offset:0\n"
    "ds_read_b128 v[20:23], v0 offset:31744\n"
    "s_waitcnt lgkmcnt(3)\n"
    "v_mfma_f32_16x16x32_bf16 v[56:59], v[220:223], v[24:27], v[56:59]\n"
    "v_permlane32_swap_b32 v48, v50\n"
    "v_permlane32_swap_b32 v49, v51\n"
    "s_mov_b32 exec_hi, 0\n"
    "buffer_store_dwordx4 v[48:51], v0, s[24:27], s49 offen sc1\n"
    "s_mov_b32 exec_hi, -1\n"
    "s_add_u32 s49, s49, 45056\n"
    "v_mfma_f32_16x16x32_bf16 v[60:63], v[224:227], v[24:27], v[60:63]\n"
    "v_mfma_f32_16x16x32_bf16 v[28:31], v[228:231], v[24:27], v[28:31]\n"
    "s_waitcnt lgkmcnt(1)\n"
    "v_cmp_gt_u32 vcc, 19, v9\n"
    "s_cbranch_vccnz .Ls4_cs44\n"
    ".Ls4_cb45:\n"
    "ds_read_b128 v[24:27], v0 offset:32768\n"
    "v_mfma_f32_16x16x32_bf16 v[56:59], v[232:235], v[16:19], v[56:59]\n"
    "v_mfma_f32_16x16x32_bf16 v[60:63], v[236:239], v[16:19], v[60:63]\n"
    "v_mfma_f32_16x16x32_bf16 v[28:31], v[240:243], v[16:19], v[28:31]\n"
    "ds_read_b128 v[16:19], v0 offset:33792\n"
    "s_waitcnt lgkmcnt(2)\n"
    "v_mfma_f32_16x16x32_bf16 v[56:59], v[244:247], v[20:23], v[56:59]\n"
    "v_mfma_f32_16x16x32_bf16 v[60:63], v[248:251], v[20:23], v[60:63]\n"
    "v_mfma_f32_16x16x32_bf16 v[28:31], v[252:255], v[20:23], v[28:31]\n"
    "v_add_u32 v12, 65536, v0\n"
    "ds_read_b128 v[12:15], v12 offset:1024\n"
    "ds_read_b128 v[20:23], v0 offset:34816\n"
    "s_waitcnt lgkmcnt(3)\n"
    "v_mfma_f32_16x16x32_bf16 v[44:47], v[64:67], v[24:27], 0\n"
    "v_mfma_f32_16x16x32_bf16 v[48:51], v[68:71], v[24:27], 0\n"
    "v_mfma_f32_16x16x32_bf16 v[52:55], v[72:75], v[24:27], 0\n"
    "ds_read_b128 v[24:27], v0 offset:35840\n"
    "s_waitcnt lgkmcnt(3)\n"
    "v_mfma_f32_16x16x32_bf16 v[44:47], v[76:79], v[16:19], v[44:47]\n"
    "s_waitcnt lgkmcnt(2)\n"
    "v_fmac_f32 v56, v32, v12\n"
    "v_fmac_f32 v57, v33, v12\n"
    "v_mfma_f32_16x16x32_bf16 v[48:51], v[80:83], v[16:19], v[48:51]\n"
    "v_fmac_f32 v58, v34, v12\n"
    "v_fmac_f32 v59, v35, v12\n"
    "v_mul_f32 v6, v56, v56\n"
    "v_mfma_f32_16x16x32_bf16 v[52:55], v[84:87], v[16:19], v[52:55]\n"
    "v_mul_f32 v7, v57, v57\n"
    "v_mul_f32 v10, v58, v58\n"
    "v_mul_f32 v11, v59, v59\n"
    "ds_read_b128 v[16:19], v0 offset:36864\n"
    "s_waitcnt lgkmcnt(2)\n"
    "v_mfma_f32_16x16x32_bf16 v[44:47], v[88:91], v[20:23], v[44:47]\n"
    "v_fma_f32 v6, v6, v14, v15\n"
    "v_fma_f32 v7, v7, v14, v15\n"
    "v_fma_f32 v10, v10, v14, v15\n"
    "v_mfma_f32_16x16x32_bf16 v[48:51], v[92:95], v[20:23], v[48:51]\n"
    "v_fma_f32 v11, v11, v14, v15\n"
    "v_mul_f32 v6, v6, v56\n"
    "v_mul_f32 v7, v7, v57\n"
    "v_mfma_f32_16x16x32_bf16 v[52:55], v[96:99], v[20:23], v[52:55]\n"
    "v_mul_f32 v10, v10, v58\n"
    "v_mul_f32 v11, v11, v59\n"
    "v_exp_f32 v6, v6\n"
    "ds_read_b128 v[20:23], v0 offset:37888\n"
    "s_waitcnt lgkmcnt(2)\n"
    "v_mfma_f32_16x16x32_bf16 v[44:47], v[100:103], v[24:27], v[44:47]\n"
    "v_exp_f32 v7, v7\n"
    "v_exp_f32 v10, v10\n"
    "v_exp_f32 v11, v11\n"
    "v_mfma_f32_16x16x32_bf16 v[48:51], v[104:107], v[24:27], v[48:51]\n"
    "v_fma_f32 v6, v6, v13, v13\n"
    "v_fma_f32 v7, v7, v13, v13\n"
    "v_mfma_f32_16x16x32_bf16 v[52:55], v[108:111], v[24:27], v[52:55]\n"
    "v_fma_f32 v10, v10, v13, v13\n"
    "v_fma_f32 v11, v11, v13, v13\n"
    "v_rcp_f32 v6, v6\n"
    "ds_read_b128 v[24:27], v0 offset:38912\n"
    "s_waitcnt lgkmcnt(2)\n"
    "v_mfma_f32_16x16x32_bf16 v[44:47], v[112:115], v[16:19], v[44:47]\n"
    "v_rcp_f32 v7, v7\n"
    "v_rcp_f32 v10, v10\n"
    "v_rcp_f32 v11, v11\n"
    "v_mfma_f32_16x16x32_bf16 v[48:51], v[116:119], v[16:19], v[48:51]\n"
    "v_mul_f32 v56, v56, v6\n"
    "v_mul_f32 v57, v57, v7\n"
    "v_mul_f32 v58, v58, v10\n"
    "v_mfma_f32_16x16x32_bf16 v[52:55], v[120:123], v[16:19], v[52:55]\n"
    "v_mul_f32 v59, v59, v11\n"
    "v_cvt_pk_bf16_f32 v56, v56, v57\n"
    "v_cvt_pk_bf16_f32 v57, v58, v59\n"
    "ds_read_b128 v[16:19], v0 offset:39936\n"
    "s_waitcnt lgkmcnt(2)\n"
    "v_mfma_f32_16x16x32_bf16 v[44:47], v[124:127], v[20:23], v[44:47]\n"
    "v_fmac_f32 v60, v36, v12\n"
    "v_fmac_f32 v61, v37, v12\n"
    "v_fmac_f32 v62, v38, v12\n"
    "v_mfma_f32_16x16x32_bf16 v[48:51], v[128:131], v[20:23], v[48:51]\n"
    "v_fmac_f32 v63, v39, v12\n"
    "v_mul_f32 v6, v60, v60\n"
    "v_mul_f32 v7, v61, v61\n"
    "v_mfma_f32_16x16x32_bf16 v[52:55], v[132:135], v[20:23], v[52:55]\n"
    "v_mul_f32 v10, v62, v62\n"
    "v_mul_f32 v11, v63, v63\n"
    "ds_read_b128 v[20:23], v0 offset:40960\n"
    "s_waitcnt lgkmcnt(2)\n"
    "v_mfma_f32_16x16x32_bf16 v[44:47], v[136:139], v[24:27], v[44:47]\n"
    "v_fma_f32 v6, v6, v14, v15\n"
    "v_fma_f32 v7, v7, v14, v15\n"
    "v_fma_f32 v10, v10, v14, v15\n"
    "v_mfma_f32_16x16x32_bf16 v[48:51], v[140:143], v[24:27], v[48:51]\n"
    "v_fma_f32 v11, v11, v14, v15\n"
    "v_mul_f32 v6, v6, v60\n"
    "v_mul_f32 v7, v7, v61\n"
    "v_mfma_f32_16x16x32_bf16 v[52:55], v[144:147], v[24:27], v[52:55]\n"
    "v_mul_f32 v10, v10, v62\n"
    "v_mul_f32 v11, v11, v63\n"
    "v_exp_f32 v6, v6\n"
    "ds_read_b128 v[24:27], v0 offset:41984\n"
    "s_waitcnt lgkmcnt(2)\n"
    "v_mfma_f32_16x16x32_bf16 v[44:47], v[148:151], v[16:19], v[44:47]\n"
    "v_exp_f32 v7, v7\n"
    "v_exp_f32 v10, v10\n"
    "v_exp_f32 v11, v11\n"
    "v_mfma_f32_16x16x32_bf16 v[48:51], v[152:155], v[16:19], v[48:51]\n"
    "v_fma_f32 v6, v6, v13, v13\n"
    "v_fma_f32 v7, v7, v13, v13\n"
    "v_fma_f32 v10, v10, v13, v13\n"
    "v_mfma_f32_16x16x32_bf16 v[52:55], v[156:159], v[16:19], v[52:55]\n"
    "v_fma_f32 v11, v11, v13, v13\n"
    "v_rcp_f32 v6, v6\n"
    "v_rcp_f32 v7, v7\n"
    "ds_read_b128 v[16:19], v0 offset:43008\n"
    "s_waitcnt lgkmcnt(2)\n"
    "v_mfma_f32_16x16x32_bf16 v[44:47], v[160:163], v[20:23], v[44:47]\n"
    "v_rcp_f32 v10, v10\n"
    "v_rcp_f32 v11, v11\n"
    "v_mul_f32 v60, v60, v6\n"
    "v_mfma_f32_16x16x32_bf16 v[48:51], v[164:167], v[20:23], v[48:51]\n"
    "v_mul_f32 v61, v61, v7\n"
    "v_mul_f32 v62, v62, v10\n"
    "v_mfma_f32_16x16x32_bf16 v[52:55], v[168:171], v[20:23], v[52:55]\n"
    "v_mul_f32 v63, v63, v11\n"
    "v_cvt_pk_bf16_f32 v58, v60, v61\n"
    "v_cvt_pk_bf16_f32 v59, v62, v63\n"
    "ds_read_b128 v[20:23], v0 offset:44032\n"
    "s_waitcnt lgkmcnt(2)\n"
    "v_mfma_f32_16x16x32_bf16 v[44:47], v[172:175], v[24:27], v[44:47]\n"
    "buffer_store_dwordx4 v[56:59], v0, s[24:27], s48 offen sc1\n"
    "s_add_u32 s48, s48, 45056\n"
    "v_fmac_f32 v28, v40, v12\n"
    "v_fmac_f32 v29, v41, v12\n"
    "v_mfma_f32_16x16x32_bf16 v[48:51], v[176:179], v[24:27], v[48:51]\n"
    "v_fmac_f32 v30, v42, v12\n"
    "v_fmac_f32 v31, v43, v12\n"
    "v_mul_f32 v6, v28, v28\n"
    "v_mfma_f32_16x16x32_bf16 v[52:55], v[180:183], v[24:27], v[52:55]\n"
    "v_mul_f32 v7, v29, v29\n"
    "v_mul_f32 v10, v30, v30\n"
    "v_mul_f32 v11, v31, v31\n"
    "s_waitcnt vmcnt(3)\n"
    "ds_inc_u32 v2, v2\n"
    "s_mov_b64 exec, 0xff\n"
    "s_waitcnt vmcnt(1)\n"
    "ds_inc_rtn_u32 v9, v3, v2 offset:64\n"
    "s_mov_b64 exec, -1\n"
    "ds_read_b128 v[24:27], v0 offset:45056\n"
    "s_waitcnt lgkmcnt(4)\n"
    "v_mfma_f32_16x16x32_bf16 v[44:47], v[184:187], v[16:19], v[44:47]\n"
    "v_fma_f32 v6, v6, v14, v15\n"
    "v_fma_f32 v7, v7, v14, v15\n"
    "v_fma_f32 v10, v10, v14, v15\n"
    "v_mfma_f32_16x16x32_bf16 v[48:51], v[188:191], v[16:19], v[48:51]\n"
    "v_fma_f32 v11, v11, v14, v15\n"
    "v_mul_f32 v6, v6, v28\n"
    "v_mul_f32 v7, v7, v29\n"
    "v_mfma_f32_16x16x32_bf16 v[52:55], v[192:195], v[16:19], v[52:55]\n"
    "v_mul_f32 v10, v10, v30\n"
    "v_mul_f32 v11, v11, v31\n"
    "ds_read_b128 v[16:19], v0 offset:46080\n"
    "s_waitcnt lgkmcnt(4)\n"
    "v_mfma_f32_16x16x32_bf16 v[44:47], v[196:199], v[20:23], v[44:47]\n"
    "v_exp_f32 v6, v6\n"
    "v_exp_f32 v7, v7\n"
    "v_exp_f32 v10, v10\n"
    "v_mfma_f32_16x16x32_bf16 v[48:51], v[200:203], v[20:23], v[48:51]\n"
    "v_exp_f32 v11, v11\n"
    "v_fma_f32 v6, v6, v13, v13\n"
    "v_fma_f32 v7, v7, v13, v13\n"
    "v_mfma_f32_16x16x32_bf16 v[52:55], v[204:207], v[20:23], v[52:55]\n"
    "v_fma_f32 v10, v10, v13, v13\n"
    "v_fma_f32 v11, v11, v13, v13\n"
    "v_rcp_f32 v6, v6\n"
    "s_mov_b64 exec, 0xff\n"
    "s_waitcnt lgkmcnt(2)\n"
    "v_cmpx_eq_u32 vcc, 3, v9\n"
    "buffer_store_dwordx4 v[4:7], v0, s[24:27], s50 offen sc1\n"
    "s_mov_b64 exec, -1\n"
    "s_add_u32 s50, s50, 1024\n"
    "ds_read_b128 v[20:23], v0 offset:47104\n"
    "s_waitcnt lgkmcnt(2)\n"
    "v_mfma_f32_16x16x32_bf16 v[44:47], v[208:211], v[24:27], v[44:47]\n"
    "v_rcp_f32 v7, v7\n"
    "v_rcp_f32 v10, v10\n"
    "v_rcp_f32 v11, v11\n"
    "v_mfma_f32_16x16x32_bf16 v[48:51], v[212:215], v[24:27], v[48:51]\n"
    "v_mul_f32 v28, v28, v6\n"
    "v_mul_f32 v29, v29, v7\n"
    "v_mul_f32 v30, v30, v10\n"
    "v_mfma_f32_16x16x32_bf16 v[52:55], v[216:219], v[24:27], v[52:55]\n"
    "v_mul_f32 v31, v31, v11\n"
    "v_cvt_pk_bf16_f32 v60, v28, v29\n"
    "v_cvt_pk_bf16_f32 v61, v30, v31\n"
    "ds_read_b32 v9, v1 offset:0\n"
    "ds_read_b128 v[24:27], v0 offset:48128\n"
    "s_waitcnt lgkmcnt(3)\n"
    "v_mfma_f32_16x16x32_bf16 v[44:47], v[220:223], v[16:19], v[44:47]\n"
    "v_permlane32_swap_b32 v60, v62\n"
    "v_permlane32_swap_b32 v61, v63\n"
    "s_mov_b32 exec_hi, 0\n"
    "buffer_store_dwordx4 v[60:63], v0, s[24:27], s49 offen sc1\n"
    "s_mov_b32 exec_hi, -1\n"
    "s_add_u32 s49, s49, 45056\n"
    "v_mfma_f32_16x16x32_bf16 v[48:51], v[224:227], v[16:19], v[48:51]\n"
    "v_mfma_f32_16x16x32_bf16 v[52:55], v[228:231], v[16:19], v[52:55]\n"
    "s_waitcnt lgkmcnt(1)\n"
    "v_cmp_gt_u32 vcc, 20, v9\n"
    "s_cbranch_vccnz .Ls4_cs46\n"
    ".Ls4_cb47:\n"
    "ds_read_b128 v[16:19], v0 offset:49152\n"
    "v_mfma_f32_16x16x32_bf16 v[44:47], v[232:235], v[20:23], v[44:47]\n"
    "v_mfma_f32_16x16x32_bf16 v[48:51], v[236:239], v[20:23], v[48:51]\n"
    "v_mfma_f32_16x16x32_bf16 v[52:55], v[240:243], v[20:23], v[52:55]\n"
    "ds_read_b128 v[20:23], v0 offset:50176\n"
    "s_waitcnt lgkmcnt(2)\n"
    "v_mfma_f32_16x16x32_bf16 v[44:47], v[244:247], v[24:27], v[44:47]\n"
    "v_mfma_f32_16x16x32_bf16 v[48:51], v[248:251], v[24:27], v[48:51]\n"
    "v_mfma_f32_16x16x32_bf16 v[52:55], v[252:255], v[24:27], v[52:55]\n"
    "v_add_u32 v12, 65536, v0\n"
    "ds_read_b128 v[12:15], v12 offset:2048\n"
    "ds_read_b128 v[24:27], v0 offset:51200\n"
    "s_waitcnt lgkmcnt(3)\n"
    "v_mfma_f32_16x16x32_bf16 v[56:59], v[64:67], v[16:19], 0\n"
    "v_mfma_f32_16x16x32_bf16 v[60:63], v[68:71], v[16:19], 0\n"
    "v_mfma_f32_16x16x32_bf16 v[28:31], v[72:75], v[16:19], 0\n"
    "ds_read_b128 v[16:19], v0 offset:52224\n"
    "s_waitcnt lgkmcnt(3)\n"
    "v_mfma_f32_16x16x32_bf16 v[56:59], v[76:79], v[20:23], v[56:59]\n"
    "s_waitcnt lgkmcnt(2)\n"
    "v_fmac_f32 v44, v32, v12\n"
    "v_fmac_f32 v45, v33, v12\n"
    "v_mfma_f32_16x16x32_bf16 v[60:63], v[80:83], v[20:23], v[60:63]\n"
    "v_fmac_f32 v46, v34, v12\n"
    "v_fmac_f32 v47, v35, v12\n"
    "v_mul_f32 v6, v44, v44\n"
    "v_mfma_f32_16x16x32_bf16 v[28:31], v[84:87], v[20:23], v[28:31]\n"
    "v_mul_f32 v7, v45, v45\n"
    "v_mul_f32 v10, v46, v46\n"
    "v_mul_f32 v11, v47, v47\n"
    "ds_read_b128 v[20:23], v0 offset:53248\n"
    "s_waitcnt lgkmcnt(2)\n"
    "v_mfma_f32_16x16x32_bf16 v[56:59], v[88:91], v[24:27], v[56:59]\n"
    "v_fma_f32 v6, v6, v14, v15\n"
    "v_fma_f32 v7, v7, v14, v15\n"
    "v_fma_f32 v10, v10, v14, v15\n"
    "v_mfma_f32_16x16x32_bf16 v[60:63], v[92:95], v[24:27], v[60:63]\n"
    "v_fma_f32 v11, v11, v14, v15\n"
    "v_mul_f32 v6, v6, v44\n"
    "v_mul_f32 v7, v7, v45\n"
    "v_mfma_f32_16x16x32_bf16 v[28:31], v[96:99], v[24:27], v[28:31]\n"
    "v_mul_f32 v10, v10, v46\n"
    "v_mul_f32 v11, v11, v47\n"
    "v_exp_f32 v6, v6\n"
    "ds_read_b128 v[24:27], v0 offset:54272\n"
    "s_waitcnt lgkmcnt(2)\n"
    "v_mfma_f32_16x16x32_bf16 v[56:59], v[100:103], v[16:19], v[56:59]\n"
    "v_exp_f32 v7, v7\n"
    "v_exp_f32 v10, v10\n"
    "v_exp_f32 v11, v11\n"
    "v_mfma_f32_16x16x32_bf16 v[60:63], v[104:107], v[16:19], v[60:63]\n"
    "v_fma_f32 v6, v6, v13, v13\n"
    "v_fma_f32 v7, v7, v13, v13\n"
    "v_mfma_f32_16x16x32_bf16 v[28:31], v[108:111], v[16:19], v[28:31]\n"
    "v_fma_f32 v10, v10, v13, v13\n"
    "v_fma_f32 v11, v11, v13, v13\n"
    "v_rcp_f32 v6, v6\n"
    "ds_read_b128 v[16:19], v0 offset:55296\n"
    "s_waitcnt lgkmcnt(2)\n"
    "v_mfma_f32_16x16x32_bf16 v[56:59], v[112:115], v[20:23], v[56:59]\n"
    "v_rcp_f32 v7, v7\n"
    "v_rcp_f32 v10, v10\n"
    "v_rcp_f32 v11, v11\n"
    "v_mfma_f32_16x16x32_bf16 v[60:63], v[116:119], v[20:23], v[60:63]\n"
    "v_mul_f32 v44, v44, v6\n"
    "v_mul_f32 v45, v45, v7\n"
    "v_mul_f32 v46, v46, v10\n"
    "v_mfma_f32_16x16x32_bf16 v[28:31], v[120:123], v[20:23], v[28:31]\n"
    "v_mul_f32 v47, v47, v11\n"
    "v_cvt_pk_bf16_f32 v44, v44, v45\n"
    "v_cvt_pk_bf16_f32 v45, v46, v47\n"
    "ds_read_b128 v[20:23], v0 offset:56320\n"
    "s_waitcnt lgkmcnt(2)\n"
    "v_mfma_f32_16x16x32_bf16 v[56:59], v[124:127], v[24:27], v[56:59]\n"
    "v_fmac_f32 v48, v36, v12\n"
    "v_fmac_f32 v49, v37, v12\n"
    "v_fmac_f32 v50, v38, v12\n"
    "v_mfma_f32_16x16x32_bf16 v[60:63], v[128:131], v[24:27], v[60:63]\n"
    "v_fmac_f32 v51, v39, v12\n"
    "v_mul_f32 v6, v48, v48\n"
    "v_mul_f32 v7, v49, v49\n"
    "v_mfma_f32_16x16x32_bf16 v[28:31], v[132:135], v[24:27], v[28:31]\n"
    "v_mul_f32 v10, v50, v50\n"
    "v_mul_f32 v11, v51, v51\n"
    "ds_read_b128 v[24:27], v0 offset:57344\n"
    "s_waitcnt lgkmcnt(2)\n"
    "v_mfma_f32_16x16x32_bf16 v[56:59], v[136:139], v[16:19], v[56:59]\n"
    "v_fma_f32 v6, v6, v14, v15\n"
    "v_fma_f32 v7, v7, v14, v15\n"
    "v_fma_f32 v10, v10, v14, v15\n"
    "v_mfma_f32_16x16x32_bf16 v[60:63], v[140:143], v[16:19], v[60:63]\n"
    "v_fma_f32 v11, v11, v14, v15\n"
    "v_mul_f32 v6, v6, v48\n"
    "v_mul_f32 v7, v7, v49\n"
    "v_mfma_f32_16x16x32_bf16 v[28:31], v[144:147], v[16:19], v[28:31]\n"
    "v_mul_f32 v10, v10, v50\n"
    "v_mul_f32 v11, v11, v51\n"
    "v_exp_f32 v6, v6\n"
    "ds_read_b128 v[16:19], v0 offset:58368\n"
    "s_waitcnt lgkmcnt(2)\n"
    "v_mfma_f32_16x16x32_bf16 v[56:59], v[148:151], v[20:23], v[56:59]\n"
    "v_exp_f32 v7, v7\n"
    "v_exp_f32 v10, v10\n"
    "v_exp_f32 v11, v11\n"
    "v_mfma_f32_16x16x32_bf16 v[60:63], v[152:155], v[20:23], v[60:63]\n"
    "v_fma_f32 v6, v6, v13, v13\n"
    "v_fma_f32 v7, v7, v13, v13\n"
    "v_fma_f32 v10, v10, v13, v13\n"
    "v_mfma_f32_16x16x32_bf16 v[28:31], v[156:159], v[20:23], v[28:31]\n"
    "v_fma_f32 v11, v11, v13, v13\n"
    "v_rcp_f32 v6, v6\n"
    "v_rcp_f32 v7, v7\n"
    "ds_read_b128 v[20:23], v0 offset:59392\n"
    "s_waitcnt lgkmcnt(2)\n"
    "v_mfma_f32_16x16x32_bf16 v[56:59], v[160:163], v[24:27], v[56:59]\n"
    "v_rcp_f32 v10, v10\n"
    "v_rcp_f32 v11, v11\n"
    "v_mul_f32 v48, v48, v6\n"
    "v_mfma_f32_16x16x32_bf16 v[60:63], v[164:167], v[24:27], v[60:63]\n"
    "v_mul_f32 v49, v49, v7\n"
    "v_mul_f32 v50, v50, v10\n"
    "v_mfma_f32_16x16x32_bf16 v[28:31], v[168:171], v[24:27], v[28:31]\n"
    "v_mul_f32 v51, v51, v11\n"
    "v_cvt_pk_bf16_f32 v46, v48, v49\n"
    "v_cvt_pk_bf16_f32 v47, v50, v51\n"
    "ds_read_b128 v[24:27], v0 offset:60416\n"
    "s_waitcnt lgkmcnt(2)\n"
    "v_mfma_f32_16x16x32_bf16 v[56:59], v[172:175], v[16:19], v[56:59]\n"
    "buffer_store_dwordx4 v[44:47], v0, s[24:27], s48 offen sc1\n"
    "s_add_u32 s48, s48, 45056\n"
    "v_fmac_f32 v52, v40, v12\n"
    "v_fmac_f32 v53, v41, v12\n"
    "v_mfma_f32_16x16x32_bf16 v[60:63], v[176:179], v[16:19], v[60:63]\n"
    "v_fmac_f32 v54, v42, v12\n"
    "v_fmac_f32 v55, v43, v12\n"
    "v_mul_f32 v6, v52, v52\n"
    "v_mfma_f32_16x16x32_bf16 v[28:31], v[180:183], v[16:19], v[28:31]\n"
    "v_mul_f32 v7, v53, v53\n"
    "v_mul_f32 v10, v54, v54\n"
    "v_mul_f32 v11, v55, v55\n"
    "s_mov_b64 exec, 0xff\n"
    "s_waitcnt vmcnt(1)\n"
    "ds_inc_rtn_u32 v9, v3, v2 offset:96\n"
    "s_mov_b64 exec, -1\n"
    "ds_read_b128 v[16:19], v0 offset:61440\n"
    "s_waitcnt lgkmcnt(3)\n"
    "v_mfma_f32_16x16x32_bf16 v[56:59], v[184:187], v[20:23], v[56:59]\n"
    "v_fma_f32 v6, v6, v14, v15\n"
    "v_fma_f32 v7, v7, v14, v15\n"
    "v_fma_f32 v10, v10, v14, v15\n"
    "v_mfma_f32_16x16x32_bf16 v[60:63], v[188:191], v[20:23], v[60:63]\n"
    "v_fma_f32 v11, v11, v14, v15\n"
    "v_mul_f32 v6, v6, v52\n"
    "v_mul_f32 v7, v7, v53\n"
    "v_mfma_f32_16x16x32_bf16 v[28:31], v[192:195], v[20:23], v[28:31]\n"
    "v_mul_f32 v10, v10, v54\n"
    "v_mul_f32 v11, v11, v55\n"
    "ds_read_b128 v[20:23], v0 offset:62464\n"
    "s_waitcnt lgkmcnt(3)\n"
    "v_mfma_f32_16x16x32_bf16 v[56:59], v[196:199], v[24:27], v[56:59]\n"
    "v_exp_f32 v6, v6\n"
    "v_exp_f32 v7, v7\n"
    "v_exp_f32 v10, v10\n"
    "v_mfma_f32_16x16x32_bf16 v[60:63], v[200:203], v[24:27], v[60:63]\n"
    "v_exp_f32 v11, v11\n"
    "v_fma_f32 v6, v6, v13, v13\n"
    "v_fma_f32 v7, v7, v13, v13\n"
    "v_mfma_f32_16x16x32_bf16 v[28:31], v[204:207], v[24:27], v[28:31]\n"
    "v_fma_f32 v10, v10, v13, v13\n"
    "v_fma_f32 v11, v11, v13, v13\n"
    "v_rcp_f32 v6, v6\n"
    "s_mov_b64 exec, 0xff\n"
    "s_waitcnt lgkmcnt(2)\n"
    "v_cmpx_eq_u32 vcc, 3, v9\n"
    "buffer_store_dwordx4 v[4:7], v0, s[24:27], s50 offen sc1\n"
    "s_mov_b64 exec, -1\n"
    "s_add_u32 s50, s50, 1024\n"
    "ds_read_b128 v[24:27], v0 offset:63488\n"
    "s_waitcnt lgkmcnt(2)\n"
    "v_mfma_f32_16x16x32_bf16 v[56:59], v[208:211], v[16:19], v[56:59]\n"
    "v_rcp_f32 v7, v7\n"
    "v_rcp_f32 v10, v10\n"
    "v_rcp_f32 v11, v11\n"
    "v_mfma_f32_16x16x32_bf16 v[60:63], v[212:215], v[16:19], v[60:63]\n"
    "v_mul_f32 v52, v52, v6\n"
    "v_mul_f32 v53, v53, v7\n"
    "v_mul_f32 v54, v54, v10\n"
    "v_mfma_f32_16x16x32_bf16 v[28:31], v[216:219], v[16:19], v[28:31]\n"
    "v_mul_f32 v55, v55, v11\n"
    "v_cvt_pk_bf16_f32 v48, v52, v53\n"
    "v_cvt_pk_bf16_f32 v49, v54, v55\n"
    "ds_read_b128 v[16:19], v0 offset:64512\n"
    "s_waitcnt lgkmcnt(2)\n"
    "v_mfma_f32_16x16x32_bf16 v[56:59], v[220:223], v[20:23], v[56:59]\n"
    "v_permlane32_swap_b32 v48, v50\n"
    "v_permlane32_swap_b32 v49, v51\n"
    "s_mov_b32 exec_hi, 0\n"
    "buffer_store_dwordx4 v[48:51], v0, s[24:27], s49 offen sc1\n"
    "s_mov_b32 exec_hi, -1\n"
    "s_add_u32 s49, s49, 45056\n"
    "v_mfma_f32_16x16x32_bf16 v[60:63], v[224:227], v[20:23], v[60:63]\n"
    "v_mfma_f32_16x16x32_bf16 v[28:31], v[228:231], v[20:23], v[28:31]\n"
    "s_waitcnt lgkmcnt(1)\n"
    "v_mfma_f32_16x16x32_bf16 v[56:59], v[232:235], v[24:27], v[56:59]\n"
    "v_mfma_f32_16x16x32_bf16 v[60:63], v[236:239], v[24:27], v[60:63]\n"
    "v_mfma_f32_16x16x32_bf16 v[28:31], v[240:243], v[24:27], v[28:31]\n"
    "s_waitcnt lgkmcnt(0)\n"
    "v_mfma_f32_16x16x32_bf16 v[56:59], v[244:247], v[16:19], v[56:59]\n"
    "v_mfma_f32_16x16x32_bf16 v[60:63], v[248:251], v[16:19], v[60:63]\n"
    "v_mfma_f32_16x16x32_bf16 v[28:31], v[252:255], v[16:19], v[28:31]\n"
    "v_add_u32 v12, 65536, v0\n"
    "ds_read_b128 v[12:15], v12 offset:3072\n"
    "s_waitcnt lgkmcnt(0)\n"
    "s_nop 2\n"
    "v_fmac_f32 v56, v32, v12\n"
    "v_fmac_f32 v57, v33, v12\n"
    "v_fmac_f32 v58, v34, v12\n"
    "v_fmac_f32 v59, v35, v12\n"
    "v_fmac_f32 v60, v36, v12\n"
    "v_fmac_f32 v61, v37, v12\n"
    "v_fmac_f32 v62, v38, v12\n"
    "v_fmac_f32 v63, v39, v12\n"
    "v_fmac_f32 v28, v40, v12\n"
    "v_fmac_f32 v29, v41, v12\n"
    "v_fmac_f32 v30, v42, v12\n"
    "v_fmac_f32 v31, v43, v12\n"
    "v_mul_f32 v16, v56, v56\n"
    "v_mul_f32 v17, v57, v57\n"
    "v_mul_f32 v18, v58, v58\n"
    "v_mul_f32 v19, v59, v59\n"
    "v_mul_f32 v20, v60, v60\n"
    "v_mul_f32 v21, v61, v61\n"
    "v_mul_f32 v22, v62, v62\n"
    "v_mul_f32 v23, v63, v63\n"
    "v_mul_f32 v24, v28, v28\n"
    "v_mul_f32 v25, v29, v29\n"
    "v_mul_f32 v26, v30, v30\n"
    "v_mul_f32 v27, v31, v31\n"
    "v_fma_f32 v16, v16, v14, v15\n"
    "v_fma_f32 v17, v17, v14, v15\n"
    "v_fma_f32 v18, v18, v14, v15\n"
    "v_fma_f32 v19, v19, v14, v15\n"
    "v_fma_f32 v20, v20, v14, v15\n"
    "v_fma_f32 v21, v21, v14, v15\n"
    "v_fma_f32 v22, v22, v14, v15\n"
    "v_fma_f32 v23, v23, v14, v15\n"
    "v_fma_f32 v24, v24, v14, v15\n"
    "v_fma_f32 v25, v25, v14, v15\n"
    "v_fma_f32 v26, v26, v14, v15\n"
    "v_fma_f32 v27, v27, v14, v15\n"
    "v_mul_f32 v16, v16, v56\n"
    "v_mul_f32 v17, v17, v57\n"
    "v_mul_f32 v18, v18, v58\n"
    "v_mul_f32 v19, v19, v59\n"
    "v_mul_f32 v20, v20, v60\n"
    "v_mul_f32 v21, v21, v61\n"
    "v_mul_f32 v22, v22, v62\n"
    "v_mul_f32 v23, v23, v63\n"
    "v_mul_f32 v24, v24, v28\n"
    "v_mul_f32 v25, v25, v29\n"
    "v_mul_f32 v26, v26, v30\n"
    "v_mul_f32 v27, v27, v31\n"
    "v_exp_f32 v16, v16\n"
    "v_exp_f32 v17, v17\n"
    "v_exp_f32 v18, v18\n"
    "v_exp_f32 v19, v19\n"
    "v_exp_f32 v20, v20\n"
    "v_exp_f32 v21, v21\n"
    "v_exp_f32 v22, v22\n"
    "v_exp_f32 v23, v23\n"
    "v_exp_f32 v24, v24\n"
    "v_exp_f32 v25, v25\n"
    "v_exp_f32 v26, v26\n"
    "v_exp_f32 v27, v27\n"
    "v_fma_f32 v16, v16, v13, v13\n"
    "v_fma_f32 v17, v17, v13, v13\n"
    "v_fma_f32 v18, v18, v13, v13\n"
    "v_fma_f32 v19, v19, v13, v13\n"
    "v_fma_f32 v20, v20, v13, v13\n"
    "v_fma_f32 v21, v21, v13, v13\n"
    "v_fma_f32 v22, v22, v13, v13\n"
    "v_fma_f32 v23, v23, v13, v13\n"
    "v_fma_f32 v24, v24, v13, v13\n"
    "v_fma_f32 v25, v25, v13, v13\n"
    "v_fma_f32 v26, v26, v13, v13\n"
    "v_fma_f32 v27, v27, v13, v13\n"
    "v_rcp_f32 v16, v16\n"
    "v_rcp_f32 v17, v17\n"
    "v_rcp_f32 v18, v18\n"
    "v_rcp_f32 v19, v19\n"
    "v_rcp_f32 v20, v20\n"
    "v_rcp_f32 v21, v21\n"
    "v_rcp_f32 v22, v22\n"
    "v_rcp_f32 v23, v23\n"
    "v_rcp_f32 v24, v24\n"
    "v_rcp_f32 v25, v25\n"
    "v_rcp_f32 v26, v26\n"
    "v_rcp_f32 v27, v27\n"
    "v_mul_f32 v56, v56, v16\n"
    "v_mul_f32 v57, v57, v17\n"
    "v_mul_f32 v58, v58, v18\n"
    "v_mul_f32 v59, v59, v19\n"
    "v_mul_f32 v60, v60, v20\n"
    "v_mul_f32 v61, v61, v21\n"
    "v_mul_f32 v62, v62, v22\n"
    "v_mul_f32 v63, v63, v23\n"
    "v_mul_f32 v28, v28, v24\n"
    "v_mul_f32 v29, v29, v25\n"
    "v_mul_f32 v30, v30, v26\n"
    "v_mul_f32 v31, v31, v27\n"
    "v_cvt_pk_bf16_f32 v56, v56, v57\n"
    "v_cvt_pk_bf16_f32 v57, v58, v59\n"
    "v_cvt_pk_bf16_f32 v58, v60, v61\n"
    "v_cvt_pk_bf16_f32 v59, v62, v63\n"
    "buffer_store_dwordx4 v[56:59], v0, s[24:27], s48 offen sc1\n"
    "s_add_u32 s48, s48, 45056\n"
    "v_cvt_pk_bf16_f32 v60, v28, v29\n"
    "v_cvt_pk_bf16_f32 v61, v30, v31\n"
    "s_nop 0\n"
    "v_permlane32_swap_b32 v60, v62\n"
    "v_permlane32_swap_b32 v61, v63\n"
    "s_mov_b32 exec_hi, 0\n"
    "buffer_store_dwordx4 v[60:63], v0, s[24:27], s49 offen sc1\n"
    "s_mov_b32 exec_hi, -1\n"
    "s_add_u32 s49, s49, 45056\n"
    "s_mov_b64 exec, 0xff\n"
    "s_waitcnt vmcnt(2)\n"
    "ds_inc_rtn_u32 v9, v3, v2 offset:128\n"
    "s_mov_b64 exec, -1\n"
    "s_mov_b64 exec, 0xff\n"
    "s_waitcnt lgkmcnt(0)\n"
    "v_cmpx_eq_u32 vcc, 3, v9\n"
    "buffer_store_dwordx4 v[4:7], v0, s[24:27], s50 offen sc1\n"
    "s_mov_b64 exec, -1\n"
    "s_add_u32 s50, s50, 1024\n"
    "v_lshrrev_b32 v12, 4, v0\n"
    "v_and_b32 v13, 15, v12\n"
    "v_lshrrev_b32 v14, 4, v12\n"
    "ds_read_b32 v15, v1 offset:1024\n"
    "s_lshl_b32 s89, s15, 2\n"
    "v_add_u32 v16, s89, v14\n"
    "v_and_b32 v17, 3, v13\n"
    "v_lshl_add_u32 v17, v17, 4, v16\n"
    "v_lshlrev_b32 v17, 4, v17\n"
    "v_bfe_u32 v18, v13, 2, 1\n"
    "v_lshl_add_u32 v17, v18, 3, v17\n"
    "v_lshrrev_b32 v18, 3, v13\n"
    "v_lshl_add_u32 v17, v18, 10, v17\n"
    "s_and_b32 s90, s101, 1\n"
    "s_lshl_b32 s90, s90, 13\n"
    "v_add_u32 v17, s90, v17\n"
    "ds_read_b64 v[120:121], v17 offset:0\n"
    "ds_read_b64 v[122:123], v17 offset:16384\n"
    "ds_read_b64 v[124:125], v17 offset:32768\n"
    "ds_read_b64 v[126:127], v17 offset:49152\n"
    "v_lshlrev_b32 v18, 3, v16\n"
    "v_add_u32 v18, 158864, v18\n"
    "ds_read_b64 v[144:145], v18 offset:0\n"
    "ds_read_b64 v[146:147], v18 offset:128\n"
    "ds_read_b64 v[148:149], v18 offset:256\n"
    "ds_read_b64 v[150:151], v18 offset:384\n"
    "v_lshlrev_b32 v19, 3, v13\n"
    "v_lshl_add_u32 v20, v16, 10, v19\n"
    "s_lshl_b32 s85, s13, 16\n"
    "s_add_u32 s85, s85, s77\n"
    "v_lshl_add_u32 v21, v16, 7, v19\n"
    "v_add_u32 v21, 149568, v21\n"
    "v_add_u32 v22, 159808, v19\n"
    "s_lshr_b32 s90, s15, 1\n"
    "v_and_b32 v23, 7, v13\n"
    "v_xor_b32 v23, s90, v23\n"
    "v_lshlrev_b32 v23, 4, v23\n"
    "v_lshl_add_u32 v23, v13, 9, v23\n"
    "v_and_b32 v24, 7, v16\n"
    "v_lshl_add_u32 v23, v24, 1, v23\n"
    "v_add_u32 v23, 69632, v23\n"
    "v_xor_b32 v24, 32, v23\n"
    "v_xor_b32 v25, 64, v23\n"
    "v_xor_b32 v26, 96, v23\n"
    "v_mov_b32 v208, 0\n"
    "v_mov_b32 v209, 0\n"
    "v_mov_b32 v210, 0\n"
    "v_mov_b32 v211, 0\n"
    "v_mov_b32 v212, 0\n"
    "v_mov_b32 v213, 0\n"
    "v_mov_b32 v214, 0\n"
    "v_mov_b32 v215, 0\n"
    "v_mov_b32 v216, 0\n"
    "v_mov_b32 v217, 0\n"
    "v_mov_b32 v218, 0\n"
    "v_mov_b32 v219, 0\n"
    "v_mov_b32 v220, 0\n"
    "v_mov_b32 v221, 0\n"
    "v_mov_b32 v222, 0\n"
    "v_mov_b32 v223, 0\n"
    "v_xor_b32 v28, v16, v13\n"
    "v_lshlrev_b32 v28, 4, v28\n"
    "v_lshl_add_u32 v28, v16, 8, v28\n"
    "v_add_u32 v29, 81920, v28\n"
    "s_waitcnt lgkmcnt(4)\n"
    "v_lshlrev_b32 v64, 16, v120\n"
    "v_and_b32 v65, 0xffff0000, v120\n"
    "v_lshlrev_b32 v66, 16, v121\n"
    "v_and_b32 v67, 0xffff0000, v121\n"
    "v_lshlrev_b32 v68, 16, v122\n"
    "v_and_b32 v69, 0xffff0000, v122\n"
    "v_lshlrev_b32 v70, 16, v123\n"
    "v_and_b32 v71, 0xffff0000, v123\n"
    "v_lshlrev_b32 v72, 16, v124\n"
    "v_and_b32 v73, 0xffff0000, v124\n"
    "v_lshlrev_b32 v74, 16, v125\n"
    "v_and_b32 v75, 0xffff0000, v125\n"
    "v_lshlrev_b32 v76, 16, v126\n"
    "v_and_b32 v77, 0xffff0000, v126\n"
    "v_lshlrev_b32 v78, 16, v127\n"
    "v_and_b32 v79, 0xffff0000, v127\n"
    "s_mov_b64 exec, 0xff\n"
    "s_waitcnt vmcnt(0)\n"
    "ds_inc_rtn_u32 v6, v3, v2 offset:160\n"
    "s_mov_b64 exec, -1\n"
    "s_mov_b64 exec, 0xff\n"
    "s_waitcnt lgkmcnt(0)\n"
    "v_cmpx_eq_u32 vcc, 3, v6\n"
    "buffer_store_dwordx4 v[4:7], v0, s[24:27], s50 offen sc1\n"
    "s_mov_b64 exec, -1\n"
    "s_lshl2_add_u32 s89, s12, s15\n"
    "s_cmp_eq_u32 s89, 3\n"
    "s_cbranch_scc1 .Ls4_ep50\n"
    ".Ls4_ep48:\n"
    "v_cmp_gt_u32 vcc, 8, v15\n"
    "s_cbranch_vccnz .Ls4_cs51\n"
    ".Ls4_cb52:\n"
    "ds_read_b64 v[128:129], v21 offset:0\n"
    "ds_read_b64 v[130:131], v21 offset:2048\n"
    "ds_read_b64 v[132:133], v21 offset:4096\n"
    "ds_read_b64 v[134:135], v21 offset:6144\n"
    "ds_read_b64 v[18:19], v22\n"
    "ds_read_u16_d16_hi v208, v23 offset:0\n"
    "ds_read_u16_d16_hi v209, v23 offset:128\n"
    "ds_read_u16_d16_hi v210, v23 offset:256\n"
    "ds_read_u16_d16_hi v211, v23 offset:384\n"
    "ds_read_u16_d16_hi v212, v24 offset:0\n"
    "ds_read_u16_d16_hi v213, v24 offset:128\n"
    "ds_read_u16_d16_hi v214, v24 offset:256\n"
    "ds_read_u16_d16_hi v215, v24 offset:384\n"
    "ds_read_u16_d16_hi v216, v25 offset:0\n"
    "ds_read_u16_d16_hi v217, v25 offset:128\n"
    "ds_read_u16_d16_hi v218, v25 offset:256\n"
    "ds_read_u16_d16_hi v219, v25 offset:384\n"
    "ds_read_u16_d16_hi v220, v26 offset:0\n"
    "ds_read_u16_d16_hi v221, v26 offset:128\n"
    "ds_read_u16_d16_hi v222, v26 offset:256\n"
    "ds_read_u16_d16_hi v223, v26 offset:384\n"
    "s_waitcnt lgkmcnt(15)\n"
    "v_lshlrev_b32 v112, 16, v18\n"
    "v_and_b32 v113, 0xffff0000, v18\n"
    "v_lshlrev_b32 v114, 16, v19\n"
    "v_and_b32 v115, 0xffff0000, v19\n"
    "v_lshlrev_b32 v80, 16, v128\n"
    "v_and_b32 v81, 0xffff0000, v128\n"
    "v_lshlrev_b32 v82, 16, v129\n"
    "v_and_b32 v83, 0xffff0000, v129\n"
    "v_lshlrev_b32 v84, 16, v130\n"
    "v_and_b32 v85, 0xffff0000, v130\n"
    "v_lshlrev_b32 v86, 16, v131\n"
    "v_and_b32 v87, 0xffff0000, v131\n"
    "v_lshlrev_b32 v88, 16, v132\n"
    "v_and_b32 v89, 0xffff0000, v132\n"
    "v_lshlrev_b32 v90, 16, v133\n"
    "v_and_b32 v91, 0xffff0000, v133\n"
    "v_lshlrev_b32 v92, 16, v134\n"
    "v_and_b32 v93, 0xffff0000, v134\n"
    "v_lshlrev_b32 v94, 16, v135\n"
    "v_and_b32 v95, 0xffff0000, v135\n"
    "v_fma_f32 v96, v80, v144, v145\n"
    "v_fma_f32 v97, v81, v144, v145\n"
    "v_fma_f32 v98, v82, v144, v145\n"
    "v_fma_f32 v99, v83, v144, v145\n"
    "v_fma_f32 v100, v84, v146, v147\n"
    "v_fma_f32 v101, v85, v146, v147\n"
    "v_fma_f32 v102, v86, v146, v147\n"
    "v_fma_f32 v103, v87, v146, v147\n"
    "v_fma_f32 v104, v88, v148, v149\n"
    "v_fma_f32 v105, v89, v148, v149\n"
    "v_fma_f32 v106, v90, v148, v149\n"
    "v_fma_f32 v107, v91, v148, v149\n"
    "v_fma_f32 v108, v92, v150, v151\n"
    "v_fma_f32 v109, v93, v150, v151\n"
    "v_fma_f32 v110, v94, v150, v151\n"
    "v_fma_f32 v111, v95, v150, v151\n"
    "s_waitcnt lgkmcnt(0)\n"
    "v_fma_f32 v96, v96, v112, v208\n"
    "v_fma_f32 v97, v97, v113, v209\n"
    "v_fma_f32 v98, v98, v114, v210\n"
    "v_fma_f32 v99, v99, v115, v211\n"
    "v_fma_f32 v100, v100, v112, v212\n"
    "v_fma_f32 v101, v101, v113, v213\n"
    "v_fma_f32 v102, v102, v114, v214\n"
    "v_fma_f32 v103, v103, v115, v215\n"
    "v_fma_f32 v104, v104, v112, v216\n"
    "v_fma_f32 v105, v105, v113, v217\n"
    "v_fma_f32 v106, v106, v114, v218\n"
    "v_fma_f32 v107, v107, v115, v219\n"
    "v_fma_f32 v108, v108, v112, v220\n"
    "v_fma_f32 v109, v109, v113, v221\n"
    "v_fma_f32 v110, v110, v114, v222\n"
    "v_fma_f32 v111, v111, v115, v223\n"
    "ds_read_b128 v[160:163], v29 offset:0\n"
    "ds_read_b128 v[164:167], v29 offset:4096\n"
    "ds_read_b128 v[168:171], v29 offset:8192\n"
    "ds_read_b128 v[172:175], v29 offset:12288\n"
    "s_add_u32 s89, s85, 0\n"
    "s_waitcnt lgkmcnt(2)\n"
    "v_add_f32 v64, v64, v160\n"
    "v_add_f32 v65, v65, v161\n"
    "v_add_f32 v66, v66, v162\n"
    "v_add_f32 v67, v67, v163\n"
    "v_add_f32 v64, v64, v164\n"
    "v_add_f32 v65, v65, v165\n"
    "v_add_f32 v66, v66, v166\n"
    "v_add_f32 v67, v67, v167\n"
    "s_waitcnt lgkmcnt(0)\n"
    "v_add_f32 v168, v168, v172\n"
    "v_add_f32 v169, v169, v173\n"
    "v_add_f32 v170, v170, v174\n"
    "v_add_f32 v171, v171, v175\n"
    "v_add_f32 v64, v64, v168\n"
    "v_add_f32 v65, v65, v169\n"
    "v_add_f32 v66, v66, v170\n"
    "v_add_f32 v67, v67, v171\n"
    "v_mul_f32 v160, 0xbfb8aa3b, v64\n"
    "v_mul_f32 v161, 0xbfb8aa3b, v65\n"
    "v_mul_f32 v162, 0xbfb8aa3b, v66\n"
    "v_mul_f32 v163, 0xbfb8aa3b, v67\n"
    "v_exp_f32 v160, v160\n"
    "v_exp_f32 v161, v161\n"
    "v_exp_f32 v162, v162\n"
    "v_exp_f32 v163, v163\n"
    "v_mul_f32 v64, v64, v96\n"
    "v_mul_f32 v65, v65, v97\n"
    "v_mul_f32 v66, v66, v98\n"
    "v_mul_f32 v67, v67, v99\n"
    "v_add_f32 v160, 1.0, v160\n"
    "v_add_f32 v161, 1.0, v161\n"
    "v_add_f32 v162, 1.0, v162\n"
    "v_add_f32 v163, 1.0, v163\n"
    "v_rcp_f32 v160, v160\n"
    "v_rcp_f32 v161, v161\n"
    "v_rcp_f32 v162, v162\n"
    "v_rcp_f32 v163, v163\n"
    "v_fma_f32 v64, v64, v160, v80\n"
    "v_fma_f32 v65, v65, v161, v81\n"
    "v_fma_f32 v66, v66, v162, v82\n"
    "v_fma_f32 v67, v67, v163, v83\n"
    "v_cvt_pk_bf16_f32 v176, v64, v65\n"
    "v_cvt_pk_bf16_f32 v177, v66, v67\n"
    "buffer_store_dwordx2 v[176:177], v20, s[40:43], s89 offen sc1\n"
    "ds_read_b32 v3, v1 offset:1024\n"
    "s_waitcnt lgkmcnt(0)\n"
    "v_cmp_gt_u32 vcc, 9, v3\n"
    "s_cbranch_vccnz .Ls4_cs53\n"
    ".Ls4_cb54:\n"
    "ds_read_b128 v[160:163], v29 offset:16384\n"
    "ds_read_b128 v[164:167], v29 offset:20480\n"
    "ds_read_b128 v[168:171], v29 offset:24576\n"
    "ds_read_b128 v[172:175], v29 offset:28672\n"
    "s_add_u32 s89, s85, 16384\n"
    "s_waitcnt lgkmcnt(2)\n"
    "v_add_f32 v68, v68, v160\n"
    "v_add_f32 v69, v69, v161\n"
    "v_add_f32 v70, v70, v162\n"
    "v_add_f32 v71, v71, v163\n"
    "v_add_f32 v68, v68, v164\n"
    "v_add_f32 v69, v69, v165\n"
    "v_add_f32 v70, v70, v166\n"
    "v_add_f32 v71, v71, v167\n"
    "s_waitcnt lgkmcnt(0)\n"
    "v_add_f32 v168, v168, v172\n"
    "v_add_f32 v169, v169, v173\n"
    "v_add_f32 v170, v170, v174\n"
    "v_add_f32 v171, v171, v175\n"
    "v_add_f32 v68, v68, v168\n"
    "v_add_f32 v69, v69, v169\n"
    "v_add_f32 v70, v70, v170\n"
    "v_add_f32 v71, v71, v171\n"
    "v_mul_f32 v160, 0xbfb8aa3b, v68\n"
    "v_mul_f32 v161, 0xbfb8aa3b, v69\n"
    "v_mul_f32 v162, 0xbfb8aa3b, v70\n"
    "v_mul_f32 v163, 0xbfb8aa3b, v71\n"
    "v_exp_f32 v160, v160\n"
    "v_exp_f32 v161, v161\n"
    "v_exp_f32 v162, v162\n"
    "v_exp_f32 v163, v163\n"
    "v_mul_f32 v68, v68, v100\n"
    "v_mul_f32 v69, v69, v101\n"
    "v_mul_f32 v70, v70, v102\n"
    "v_mul_f32 v71, v71, v103\n"
    "v_add_f32 v160, 1.0, v160\n"
    "v_add_f32 v161, 1.0, v161\n"
    "v_add_f32 v162, 1.0, v162\n"
    "v_add_f32 v163, 1.0, v163\n"
    "v_rcp_f32 v160, v160\n"
    "v_rcp_f32 v161, v161\n"
    "v_rcp_f32 v162, v162\n"
    "v_rcp_f32 v163, v163\n"
    "v_fma_f32 v68, v68, v160, v84\n"
    "v_fma_f32 v69, v69, v161, v85\n"
    "v_fma_f32 v70, v70, v162, v86\n"
    "v_fma_f32 v71, v71, v163, v87\n"
    "v_cvt_pk_bf16_f32 v178, v68, v69\n"
    "v_cvt_pk_bf16_f32 v179, v70, v71\n"
    "buffer_store_dwordx2 v[178:179], v20, s[40:43], s89 offen sc1\n"
    "ds_read_b32 v3, v1 offset:1024\n"
    "s_waitcnt lgkmcnt(0)\n"
    "v_cmp_gt_u32 vcc, 10, v3\n"
    "s_cbranch_vccnz .Ls4_cs55\n"
    ".Ls4_cb56:\n"
    "ds_read_b128 v[160:163], v29 offset:32768\n"
    "ds_read_b128 v[164:167], v29 offset:36864\n"
    "ds_read_b128 v[168:171], v29 offset:40960\n"
    "ds_read_b128 v[172:175], v29 offset:45056\n"
    "s_add_u32 s89, s85, 32768\n"
    "s_waitcnt lgkmcnt(2)\n"
    "v_add_f32 v72, v72, v160\n"
    "v_add_f32 v73, v73, v161\n"
    "v_add_f32 v74, v74, v162\n"
    "v_add_f32 v75, v75, v163\n"
    "v_add_f32 v72, v72, v164\n"
    "v_add_f32 v73, v73, v165\n"
    "v_add_f32 v74, v74, v166\n"
    "v_add_f32 v75, v75, v167\n"
    "s_waitcnt lgkmcnt(0)\n"
    "v_add_f32 v168, v168, v172\n"
    "v_add_f32 v169, v169, v173\n"
    "v_add_f32 v170, v170, v174\n"
    "v_add_f32 v171, v171, v175\n"
    "v_add_f32 v72, v72, v168\n"
    "v_add_f32 v73, v73, v169\n"
    "v_add_f32 v74, v74, v170\n"
    "v_add_f32 v75, v75, v171\n"
    "v_mul_f32 v160, 0xbfb8aa3b, v72\n"
    "v_mul_f32 v161, 0xbfb8aa3b, v73\n"
    "v_mul_f32 v162, 0xbfb8aa3b, v74\n"
    "v_mul_f32 v163, 0xbfb8aa3b, v75\n"
    "v_exp_f32 v160, v160\n"
    "v_exp_f32 v161, v161\n"
    "v_exp_f32 v162, v162\n"
    "v_exp_f32 v163, v163\n"
    "v_mul_f32 v72, v72, v104\n"
    "v_mul_f32 v73, v73, v105\n"
    "v_mul_f32 v74, v74, v106\n"
    "v_mul_f32 v75, v75, v107\n"
    "v_add_f32 v160, 1.0, v160\n"
    "v_add_f32 v161, 1.0, v161\n"
    "v_add_f32 v162, 1.0, v162\n"
    "v_add_f32 v163, 1.0, v163\n"
    "v_rcp_f32 v160, v160\n"
    "v_rcp_f32 v161, v161\n"
    "v_rcp_f32 v162, v162\n"
    "v_rcp_f32 v163, v163\n"
    "v_fma_f32 v72, v72, v160, v88\n"
    "v_fma_f32 v73, v73, v161, v89\n"
    "v_fma_f32 v74, v74, v162, v90\n"
    "v_fma_f32 v75, v75, v163, v91\n"
    "v_cvt_pk_bf16_f32 v180, v72, v73\n"
    "v_cvt_pk_bf16_f32 v181, v74, v75\n"
    "buffer_store_dwordx2 v[180:181], v20, s[40:43], s89 offen sc1\n"
    "ds_read_b32 v3, v1 offset:1024\n"
    "s_waitcnt lgkmcnt(0)\n"
    "v_cmp_gt_u32 vcc, 11, v3\n"
    "s_cbranch_vccnz .Ls4_cs57\n"
    ".Ls4_cb58:\n"
    "ds_read_b128 v[160:163], v29 offset:49152\n"
    "ds_read_b128 v[164:167], v29 offset:53248\n"
    "ds_read_b128 v[168:171], v29 offset:57344\n"
    "ds_read_b128 v[172:175], v29 offset:61440\n"
    "s_add_u32 s89, s85, 49152\n"
    "s_waitcnt lgkmcnt(2)\n"
    "v_add_f32 v76, v76, v160\n"
    "v_add_f32 v77, v77, v161\n"
    "v_add_f32 v78, v78, v162\n"
    "v_add_f32 v79, v79, v163\n"
    "v_add_f32 v76, v76, v164\n"
    "v_add_f32 v77, v77, v165\n"
    "v_add_f32 v78, v78, v166\n"
    "v_add_f32 v79, v79, v167\n"
    "s_waitcnt lgkmcnt(0)\n"
    "v_add_f32 v168, v168, v172\n"
    "v_add_f32 v169, v169, v173\n"
    "v_add_f32 v170, v170, v174\n"
    "v_add_f32 v171, v171, v175\n"
    "v_add_f32 v76, v76, v168\n"
    "v_add_f32 v77, v77, v169\n"
    "v_add_f32 v78, v78, v170\n"
    "v_add_f32 v79, v79, v171\n"
    "v_mul_f32 v160, 0xbfb8aa3b, v76\n"
    "v_mul_f32 v161, 0xbfb8aa3b, v77\n"
    "v_mul_f32 v162, 0xbfb8aa3b, v78\n"
    "v_mul_f32 v163, 0xbfb8aa3b, v79\n"
    "v_exp_f32 v160, v160\n"
    "v_exp_f32 v161, v161\n"
    "v_exp_f32 v162, v162\n"
    "v_exp_f32 v163, v163\n"
    "v_mul_f32 v76, v76, v108\n"
    "v_mul_f32 v77, v77, v109\n"
    "v_mul_f32 v78, v78, v110\n"
    "v_mul_f32 v79, v79, v111\n"
    "v_add_f32 v160, 1.0, v160\n"
    "v_add_f32 v161, 1.0, v161\n"
    "v_add_f32 v162, 1.0, v162\n"
    "v_add_f32 v163, 1.0, v163\n"
    "v_rcp_f32 v160, v160\n"
    "v_rcp_f32 v161, v161\n"
    "v_rcp_f32 v162, v162\n"
    "v_rcp_f32 v163, v163\n"
    "v_fma_f32 v76, v76, v160, v92\n"
    "v_fma_f32 v77, v77, v161, v93\n"
    "v_fma_f32 v78, v78, v162, v94\n"
    "v_fma_f32 v79, v79, v163, v95\n"
    "v_cvt_pk_bf16_f32 v182, v76, v77\n"
    "v_cvt_pk_bf16_f32 v183, v78, v79\n"
    "buffer_store_dwordx2 v[182:183], v20, s[40:43], s89 offen sc1\n"
    "s_endpgm\n"
    ".Ls4_cs1:\n"
    "ds_read_b32 v3, v1 offset:0\n"
    "s_waitcnt lgkmcnt(0)\n"
    "v_cmp_gt_u32 vcc, 1, v3\n"
    "s_cbranch_vccz .Ls4_cb2\n"
    "s_sub_u32 s100, s100, 1\n"
    "s_cselect_b32 s100, 0, s100\n"
    "s_cbranch_scc0 .Ls4_cs1\n"
    "s_branch .Ls4_cb2\n"
    ".Ls4_cs3:\n"
    "ds_read_b32 v3, v1 offset:0\n"
    "s_waitcnt lgkmcnt(0)\n"
    "v_cmp_gt_u32 vcc, 3, v3\n"
    "s_cbranch_vccz .Ls4_cb4\n"
    "s_sub_u32 s100, s100, 1\n"
    "s_cselect_b32 s100, 0, s100\n"
    "s_cbranch_scc0 .Ls4_cs3\n"
    "s_branch .Ls4_cb4\n"
    ".Ls4_cs5:\n"
    "ds_read_b32 v15, v1 offset:1024\n"
    "s_waitcnt lgkmcnt(0)\n"
    "v_cmp_gt_u32 vcc, 1, v15\n"
    "s_cbranch_vccz .Ls4_cb6\n"
    "s_sub_u32 s100, s100, 1\n"
    "s_cselect_b32 s100, 0, s100\n"
    "s_cbranch_scc0 .Ls4_cs5\n"
    "s_branch .Ls4_cb6\n"
    ".Ls4_cs7:\n"
    "ds_read_b32 v15, v1 offset:0\n"
    "s_waitcnt lgkmcnt(0)\n"
    "v_cmp_gt_u32 vcc, 4, v15\n"
    "s_cbranch_vccz .Ls4_cb8\n"
    "s_sub_u32 s100, s100, 1\n"
    "s_cselect_b32 s100, 0, s100\n"
    "s_cbranch_scc0 .Ls4_cs7\n"
    "s_branch .Ls4_cb8\n"
    ".Ls4_cs9:\n"
    "ds_read_b32 v3, v1 offset:0\n"
    "s_waitcnt lgkmcnt(0)\n"
    "v_cmp_gt_u32 vcc, 5, v3\n"
    "s_cbranch_vccz .Ls4_cb10\n"
    "s_sub_u32 s100, s100, 1\n"
    "s_cselect_b32 s100, 0, s100\n"
    "s_cbranch_scc0 .Ls4_cs9\n"
    "s_branch .Ls4_cb10\n"
    ".Ls4_cs11:\n"
    "ds_read_b32 v3, v1 offset:0\n"
    "s_waitcnt lgkmcnt(0)\n"
    "v_cmp_gt_u32 vcc, 6, v3\n"
    "s_cbranch_vccz .Ls4_cb12\n"
    "s_sub_u32 s100, s100, 1\n"
    "s_cselect_b32 s100, 0, s100\n"
    "s_cbranch_scc0 .Ls4_cs11\n"
    "s_branch .Ls4_cb12\n"
    ".Ls4_cs13:\n"
    "ds_read_b32 v3, v1 offset:0\n"
    "s_waitcnt lgkmcnt(0)\n"
    "v_cmp_gt_u32 vcc, 8, v3\n"
    "s_cbranch_vccz .Ls4_cb14\n"
    "s_sub_u32 s100, s100, 1\n"
    "s_cselect_b32 s100, 0, s100\n"
    "s_cbranch_scc0 .Ls4_cs13\n"
    "s_branch .Ls4_cb14\n"
    ".Ls4_cv15:\n"
    "v_cvt_pk_bf16_f32 v226, v176, v177\n"
    "v_cvt_pk_bf16_f32 v227, v178, v179\n"
    "v_cvt_pk_bf16_f32 v224, v180, v181\n"
    "v_cvt_pk_bf16_f32 v225, v182, v183\n"
    "v_cvt_pk_bf16_f32 v230, v184, v185\n"
    "v_cvt_pk_bf16_f32 v231, v186, v187\n"
    "v_cvt_pk_bf16_f32 v228, v188, v189\n"
    "v_cvt_pk_bf16_f32 v229, v190, v191\n"
    "s_branch .Ls4_cv16\n"
    ".Ls4_cs17:\n"
    "ds_read_b32 v15, v1 offset:0\n"
    "s_waitcnt lgkmcnt(0)\n"
    "v_cmp_gt_u32 vcc, 9, v15\n"
    "s_cbranch_vccz .Ls4_cb18\n"
    "s_sub_u32 s100, s100, 1\n"
    "s_cselect_b32 s100, 0, s100\n"
    "s_cbranch_scc0 .Ls4_cs17\n"
    "s_branch .Ls4_cb18\n"
    ".Ls4_ns19:\n"
    "ds_read_b32 v3, v48 offset:224\n"
    "s_waitcnt lgkmcnt(0)\n"
    "v_cmp_gt_u32 vcc, 4, v3\n"
    "s_cbranch_vccz .Ls4_no21\n"
    "s_sleep 1\n"
    "s_sub_u32 s100, s100, 1\n"
    "s_cselect_b32 s100, 0, s100\n"
    "s_cbranch_scc0 .Ls4_ns19\n"
    "s_branch .Ls4_nb20\n"
    ".Ls4_no21:\n"
    "s_branch .Ls4_nb20\n"
    ".Ls4_cs22:\n"
    "ds_read_b32 v3, v1 offset:0\n"
    "s_waitcnt lgkmcnt(0)\n"
    "v_cmp_gt_u32 vcc, 10, v3\n"
    "s_cbranch_vccz .Ls4_cb23\n"
    "s_sub_u32 s100, s100, 1\n"
    "s_cselect_b32 s100, 0, s100\n"
    "s_cbranch_scc0 .Ls4_cs22\n"
    "s_branch .Ls4_cb23\n"
    ".Ls4_ps24:\n"
    "buffer_load_dwordx2 v[6:7], v24, s[24:27], s74 offen sc1\n"
    "s_waitcnt vmcnt(0)\n"
    "v_cmp_ne_u64 vcc, s[96:97], v[6:7]\n"
    "s_cbranch_vccz .Ls4_pb25\n"
    "s_sleep 2\n"
    "s_sub_u32 s100, s100, 1\n"
    "s_cselect_b32 s100, 0, s100\n"
    "s_cbranch_scc0 .Ls4_ps24\n"
    "s_branch .Ls4_pb25\n"
    ".Ls4_cs26:\n"
    "ds_read_b32 v15, v1 offset:0\n"
    "s_waitcnt lgkmcnt(0)\n"
    "v_cmp_gt_u32 vcc, 11, v15\n"
    "s_cbranch_vccz .Ls4_cb27\n"
    "s_sub_u32 s100, s100, 1\n"
    "s_cselect_b32 s100, 0, s100\n"
    "s_cbranch_scc0 .Ls4_cs26\n"
    "s_branch .Ls4_cb27\n"
    ".Ls4_cs28:\n"
    "ds_read_b32 v3, v1 offset:0\n"
    "s_waitcnt lgkmcnt(0)\n"
    "v_cmp_gt_u32 vcc, 12, v3\n"
    "s_cbranch_vccz .Ls4_cb29\n"
    "s_sub_u32 s100, s100, 1\n"
    "s_cselect_b32 s100, 0, s100\n"
    "s_cbranch_scc0 .Ls4_cs28\n"
    "s_branch .Ls4_cb29\n"
    ".Ls4_cs30:\n"
    "ds_read_b32 v3, v1 offset:0\n"
    "s_waitcnt lgkmcnt(0)\n"
    "v_cmp_gt_u32 vcc, 13, v3\n"
    "s_cbranch_vccz .Ls4_cb31\n"
    "s_sub_u32 s100, s100, 1\n"
    "s_cselect_b32 s100, 0, s100\n"
    "s_cbranch_scc0 .Ls4_cs30\n"
    "s_branch .Ls4_cb31\n"
    ".Ls4_cs32:\n"
    "ds_read_b32 v3, v1 offset:0\n"
    "s_waitcnt lgkmcnt(0)\n"
    "v_cmp_gt_u32 vcc, 14, v3\n"
    "s_cbranch_vccz .Ls4_cb33\n"
    "s_sub_u32 s100, s100, 1\n"
    "s_cselect_b32 s100, 0, s100\n"
    "s_cbranch_scc0 .Ls4_cs32\n"
    "s_branch .Ls4_cb33\n"
    ".Ls4_ps34:\n"
    "buffer_load_dwordx2 v[6:7], v24, s[24:27], s75 offen sc1\n"
    "s_waitcnt vmcnt(0)\n"
    "v_cmp_ne_u64 vcc, s[96:97], v[6:7]\n"
    "s_cbranch_vccz .Ls4_pb35\n"
    "s_sleep 2\n"
    "s_sub_u32 s100, s100, 1\n"
    "s_cselect_b32 s100, 0, s100\n"
    "s_cbranch_scc0 .Ls4_ps34\n"
    "s_branch .Ls4_pb35\n"
    ".Ls4_cs36:\n"
    "ds_read_b32 v3, v1 offset:0\n"
    "s_waitcnt lgkmcnt(0)\n"
    "v_cmp_gt_u32 vcc, 15, v3\n"
    "s_cbranch_vccz .Ls4_cb37\n"
    "s_sub_u32 s100, s100, 1\n"
    "s_cselect_b32 s100, 0, s100\n"
    "s_cbranch_scc0 .Ls4_cs36\n"
    "s_branch .Ls4_cb37\n"
    ".Ls4_cs38:\n"
    "ds_read_b32 v3, v1 offset:0\n"
    "s_waitcnt lgkmcnt(0)\n"
    "v_cmp_gt_u32 vcc, 16, v3\n"
    "s_cbranch_vccz .Ls4_cb39\n"
    "s_sub_u32 s100, s100, 1\n"
    "s_cselect_b32 s100, 0, s100\n"
    "s_cbranch_scc0 .Ls4_cs38\n"
    "s_branch .Ls4_cb39\n"
    ".Ls4_cs40:\n"
    "ds_read_b32 v3, v1 offset:0\n"
    "s_waitcnt lgkmcnt(0)\n"
    "v_cmp_gt_u32 vcc, 17, v3\n"
    "s_cbranch_vccz .Ls4_cb41\n"
    "s_sub_u32 s100, s100, 1\n"
    "s_cselect_b32 s100, 0, s100\n"
    "s_cbranch_scc0 .Ls4_cs40\n"
    "s_branch .Ls4_cb41\n"
    ".Ls4_cs42:\n"
    "ds_read_b32 v9, v1 offset:0\n"
    "s_waitcnt lgkmcnt(0)\n"
    "v_cmp_gt_u32 vcc, 18, v9\n"
    "s_cbranch_vccz .Ls4_cb43\n"
    "s_sub_u32 s100, s100, 1\n"
    "s_cselect_b32 s100, 0, s100\n"
    "s_cbranch_scc0 .Ls4_cs42\n"
    "s_branch .Ls4_cb43\n"
    ".Ls4_cs44:\n"
    "ds_read_b32 v9, v1 offset:0\n"
    "s_waitcnt lgkmcnt(0)\n"
    "v_cmp_gt_u32 vcc, 19, v9\n"
    "s_cbranch_vccz .Ls4_cb45\n"
    "s_sub_u32 s100, s100, 1\n"
    "s_cselect_b32 s100, 0, s100\n"
    "s_cbranch_scc0 .Ls4_cs44\n"
    "s_branch .Ls4_cb45\n"
    ".Ls4_cs46:\n"
    "ds_read_b32 v9, v1 offset:0\n"
    "s_waitcnt lgkmcnt(0)\n"
    "v_cmp_gt_u32 vcc, 20, v9\n"
    "s_cbranch_vccz .Ls4_cb47\n"
    "s_sub_u32 s100, s100, 1\n"
    "s_cselect_b32 s100, 0, s100\n"
    "s_cbranch_scc0 .Ls4_cs46\n"
    "s_branch .Ls4_cb47\n"
    ".Ls4_ep50:\n"
    "v_lshlrev_b32 v30, 3, v0\n"
    "s_mov_b32 s89, 65536\n"
    "buffer_load_dwordx2 v[176:177], v30, s[24:27], s89 offen sc1\n"
    "s_mov_b32 s89, 73728\n"
    "buffer_load_dwordx2 v[178:179], v30, s[24:27], s89 offen sc1\n"
    "s_mov_b32 s89, 81920\n"
    "buffer_load_dwordx2 v[180:181], v30, s[24:27], s89 offen sc1\n"
    "s_mov_b32 s89, 90112\n"
    "buffer_load_dwordx2 v[182:183], v30, s[24:27], s89 offen sc1\n"
    "s_waitcnt vmcnt(0)\n"
    "v_cmp_ne_u64 vcc, s[96:97], v[176:177]\n"
    "v_cmp_ne_u64 s[90:91], s[96:97], v[178:179]\n"
    "s_nop 0\n"
    "s_or_b64 vcc, vcc, s[90:91]\n"
    "v_cmp_ne_u64 s[90:91], s[96:97], v[180:181]\n"
    "s_nop 0\n"
    "s_or_b64 vcc, vcc, s[90:91]\n"
    "v_cmp_ne_u64 s[90:91], s[96:97], v[182:183]\n"
    "s_nop 0\n"
    "s_or_b64 vcc, vcc, s[90:91]\n"
    "s_cbranch_vccz .Ls4_ep49\n"
    "s_sub_u32 s100, s100, 1\n"
    "s_cselect_b32 s100, 0, s100\n"
    "s_cbranch_scc0 .Ls4_ep50\n"
    "s_branch .Ls4_ep48\n"
    ".Ls4_ep49:\n"
    "s_mov_b64 exec, 1\n"
    "buffer_store_dwordx2 v[4:5], off, s[24:27], 0 sc1\n"
    "s_mov_b64 exec, -1\n"
    "s_branch .Ls4_ep48\n"
    ".Ls4_cs51:\n"
    "ds_read_b32 v15, v1 offset:1024\n"
    "s_waitcnt lgkmcnt(0)\n"
    "v_cmp_gt_u32 vcc, 8, v15\n"
    "s_cbranch_vccz .Ls4_cb52\n"
    "s_sub_u32 s100, s100, 1\n"
    "s_cselect_b32 s100, 0, s100\n"
    "s_cbranch_scc0 .Ls4_cs51\n"
    "s_branch .Ls4_cb52\n"
    ".Ls4_cs53:\n"
    "ds_read_b32 v3, v1 offset:1024\n"
    "s_waitcnt lgkmcnt(0)\n"
    "v_cmp_gt_u32 vcc, 9, v3\n"
    "s_cbranch_vccz .Ls4_cb54\n"
    "s_sub_u32 s100, s100, 1\n"
    "s_cselect_b32 s100, 0, s100\n"
    "s_cbranch_scc0 .Ls4_cs53\n"
    "s_branch .Ls4_cb54\n"
    ".Ls4_cs55:\n"
    "ds_read_b32 v3, v1 offset:1024\n"
    "s_waitcnt lgkmcnt(0)\n"
    "v_cmp_gt_u32 vcc, 10, v3\n"
    "s_cbranch_vccz .Ls4_cb56\n"
    "s_sub_u32 s100, s100, 1\n"
    "s_cselect_b32 s100, 0, s100\n"
    "s_cbranch_scc0 .Ls4_cs55\n"
    "s_branch .Ls4_cb56\n"
    ".Ls4_cs57:\n"
    "ds_read_b32 v3, v1 offset:1024\n"
    "s_waitcnt lgkmcnt(0)\n"
    "v_cmp_gt_u32 vcc, 11, v3\n"
    "s_cbranch_vccz .Ls4_cb58\n"
    "s_sub_u32 s100, s100, 1\n"
    "s_cselect_b32 s100, 0, s100\n"
    "s_cbranch_scc0 .Ls4_cs57\n"
    "s_branch .Ls4_cb58\n"
    ".Ls4_roleB:\n"
    "s_mul_i32 s84, s15, 4624\n"
    "s_add_u32 s84, s84, 131072\n"
    "v_lshl_add_u32 v30, v12, 2, s84\n"
    "v_lshl_add_u32 v28, v14, 4, s84\n"
    "v_mov_b32 v29, 272\n"
    "v_mad_u32_u24 v32, v13, v29, v28\n"
    "v_bfe_u32 v23, v13, 1, 1\n"
    "v_add3_u32 v23, v23, v13, 0\n"
    "v_lshrrev_b32 v23, 1, v23\n"
    "v_mad_u32_u24 v33, v23, v29, v28\n"
    "v_bfe_u32 v23, v13, 2, 1\n"
    "v_add3_u32 v23, v23, v13, 1\n"
    "v_lshrrev_b32 v23, 2, v23\n"
    "v_mad_u32_u24 v34, v23, v29, v28\n"
    "v_bfe_u32 v23, v13, 3, 1\n"
    "v_add3_u32 v23, v23, v13, 3\n"
    "v_lshrrev_b32 v23, 3, v23\n"
    "v_mad_u32_u24 v35, v23, v29, v28\n"
    "v_lshrrev_b32 v23, 3, v12\n"
    "v_and_b32 v28, 7, v12\n"
    "v_lshlrev_b32 v28, 4, v28\n"
    "v_lshl_add_u32 v28, v23, 10, v28\n"
    "v_lshlrev_b32 v23, 5, v14\n"
    "v_xor_b32 v36, v23, v28\n"
    "v_add_u32 v37, 7168, v36\n"
    "v_add_u32 v38, 14336, v36\n"
    "v_add_u32 v39, 21504, v36\n"
    "s_waitcnt lgkmcnt(0)\n"
    "s_add_u32 s96, s96, 0x7f4a7c15\n"
    "s_addc_u32 s97, s97, 0x9e3779b9\n"
    "v_mov_b32 v4, s96\n"
    "v_mov_b32 v5, s97\n"
    "s_lshr_b32 s89, s36, 7\n"
    "s_sub_u32 s89, 3, s89\n"
    "s_and_b32 s89, s89, 7\n"
    "s_cmp_eq_u32 s14, 7\n"
    "s_cselect_b32 s90, s89, s14\n"
    "s_cmp_eq_u32 s14, s89\n"
    "s_cselect_b32 s90, 7, s90\n"
    "s_cmp_eq_u32 s90, 7\n"
    "s_cselect_b32 s90, 4, 64\n"
    "s_mul_i32 s89, s15, 192\n"
    "v_add_u32 v228, s89, v12\n"
    "v_mul_u32_u24 v229, 2731, v228\n"
    "v_lshrrev_b32 v229, 16, v229\n"
    "v_mul_u32_u24 v230, 24, v229\n"
    "v_sub_u32 v230, v228, v230\n"
    "v_and_b32 v231, 6, v229\n"
    "v_xor_b32 v230, v230, v231\n"
    "v_mul_u32_u24 v229, 2752, v229\n"
    "v_lshl_add_u32 v224, v230, 4, v229\n"
    "v_subrev_u32 v231, s90, v230\n"
    "v_ashrrev_i32 v231, 31, v231\n"
    "v_add_u32 v231, 1, v231\n"
    "v_lshl_add_u32 v224, v231, 30, v224\n"
    "v_add_u32 v228, s89, v12\n"
    "v_add_u32 v228, 64, v228\n"
    "v_mul_u32_u24 v229, 2731, v228\n"
    "v_lshrrev_b32 v229, 16, v229\n"
    "v_mul_u32_u24 v230, 24, v229\n"
    "v_sub_u32 v230, v228, v230\n"
    "v_and_b32 v231, 6, v229\n"
    "v_xor_b32 v230, v230, v231\n"
    "v_mul_u32_u24 v229, 2752, v229\n"
    "v_lshl_add_u32 v225, v230, 4, v229\n"
    "v_subrev_u32 v231, s90, v230\n"
    "v_ashrrev_i32 v231, 31, v231\n"
    "v_add_u32 v231, 1, v231\n"
    "v_lshl_add_u32 v225, v231, 30, v225\n"
    "v_subrev_u32 v225, 1024, v225\n"
    "v_add_u32 v228, s89, v12\n"
    "v_add_u32 v228, 128, v228\n"
    "v_mul_u32_u24 v229, 2731, v228\n"
    "v_lshrrev_b32 v229, 16, v229\n"
    "v_mul_u32_u24 v230, 24, v229\n"
    "v_sub_u32 v230, v228, v230\n"
    "v_and_b32 v231, 6, v229\n"
    "v_xor_b32 v230, v230, v231\n"
    "v_mul_u32_u24 v229, 2752, v229\n"
    "v_lshl_add_u32 v226, v230, 4, v229\n"
    "v_subrev_u32 v231, s90, v230\n"
    "v_ashrrev_i32 v231, 31, v231\n"
    "v_add_u32 v231, 1, v231\n"
    "v_lshl_add_u32 v226, v231, 30, v226\n"
    "v_subrev_u32 v226, 2048, v226\n"
    "v_mov_b32 v227, 160064\n"
    "s_xor_b32 s89, s15, 1\n"
    "s_lshl_b32 s90, s89, 8\n"
    "s_add_u32 s90, s90, 162112\n"
    "v_mov_b32 v9, s90\n"
    "v_add_u32 v208, s84, v0\n"
    "s_mul_i32 s89, s89, 4624\n"
    "s_add_u32 s89, s89, 131072\n"
    "v_add_u32 v209, s89, v0\n"
    "s_lshr_b32 s86, s15, 1\n"
    "s_mul_i32 s85, s86, 9248\n"
    "s_add_u32 s85, s85, 131072\n"
    "s_lshl_b32 s89, s81, 11\n"
    "s_add_u32 s89, s89, s85\n"
    "v_lshlrev_b32 v23, 5, v13\n"
    "v_lshl_add_u32 v28, v14, 3, v23\n"
    "v_add_u32 v210, s89, v28\n"
    "v_lshl_add_u32 v211, v12, 2, s85\n"
    "s_lshl_b32 s89, s81, 12\n"
    "s_add_u32 s90, s89, s85\n"
    "v_add_u32 v215, s90, v0\n"
    "s_xor_b32 s89, s89, 4096\n"
    "s_add_u32 s90, s89, s85\n"
    "v_add_u32 v216, s90, v0\n"
    "v_and_b32 v28, 1, v14\n"
    "v_lshl_add_u32 v28, v28, 9, v23\n"
    "v_lshrrev_b32 v29, 1, v14\n"
    "v_lshl_add_u32 v217, v29, 4, v28\n"
    "v_and_b32 v28, 7, v12\n"
    "v_lshlrev_b32 v212, 7, v28\n"
    "s_lshl_b32 s90, s15, 10\n"
    "s_sub_u32 s90, 152892, s90\n"
    "v_lshlrev_b32 v28, 2, v12\n"
    "v_sub_u32 v213, s90, v28\n"
    "s_mul_i32 s90, s86, 4608\n"
    "s_mul_i32 s91, s81, 240\n"
    "s_add_u32 s91, s91, s90\n"
    "s_add_u32 s91, s91, 153664\n"
    "v_add_u32 v214, s91, v28\n"
    "v_add_u32 v28, 1, v13\n"
    "v_lshrrev_b32 v28, 1, v28\n"
    "v_lshl_add_u32 v28, v14, 2, v28\n"
    "v_lshlrev_b32 v28, 2, v28\n"
    "v_and_b32 v29, 1, v13\n"
    "v_xor_b32 v29, 1, v29\n"
    "v_mul_u32_u24 v29, 4160, v29\n"
    "s_lshl_b32 s91, s81, 8\n"
    "s_add_u32 s92, s91, s85\n"
    "v_add3_u32 v220, v28, v29, s92\n"
    "v_add_u32 v221, 1024, v220\n"
    "v_add_u32 v222, 2048, v220\n"
    "v_add_u32 v223, 3072, v220\n"
    "v_lshlrev_b32 v28, 4, v14\n"
    "v_sub_u32 v28, v28, v23\n"
    "s_add_u32 s91, s91, s90\n"
    "s_add_u32 s91, s91, 150048\n"
    "v_add_u32 v31, s91, v28\n"
    "s_waitcnt vmcnt(1)\n"
    "s_lshl_b32 s65, s14, 1\n"
    "s_getpc_b64 s[68:69]\n"
    ".Ls4_bpc:\n"
    "s_add_u32 s68, s68, .Ls4_bt0-.Ls4_bpc\n"
    "s_addc_u32 s69, s69, 0\n"
    "s_mov_b32 s72, 2\n"
    "s_lshr_b32 s74, s36, 7\n"
    "s_sub_u32 s74, 3, s74\n"
    "s_and_b32 s74, s74, 7\n"
    "s_cmp_eq_u32 s14, 7\n"
    "s_cselect_b32 s73, s74, s14\n"
    "s_cmp_eq_u32 s14, s74\n"
    "s_cselect_b32 s73, 7, s73\n"
    "s_mul_i32 s73, s73, 384\n"
    "s_mul_i32 s74, s15, 3072\n"
    "s_and_b32 s75, s101, 1\n"
    "s_lshl_b32 s75, s75, 3\n"
    "s_xor_b32 s75, s75, s65\n"
    "v_mul_f32 v22, 0x3fb8aa3b, v22\n"
    "v_exp_f32 v22, v22\n"
    "s_nop 0\n"
    "v_cvt_pk_bf16_f32 v22, v22, v22\n"
    "v_and_b32 v22, 0xffff0000, v22\n"
    "v_mul_f32 v16, v22, v16\n"
    "v_mul_f32 v17, v22, v17\n"
    "v_mul_f32 v18, v22, v18\n"
    "v_mul_f32 v19, v22, v19\n"
    "v_mul_f32 v16, 0x3fb8aa3b, v16\n"
    "v_mul_f32 v17, 0x3e22f983, v17\n"
    "v_mul_f32 v28, 0x41800000, v16\n"
    "v_mul_f32 v29, 0x41800000, v17\n"
    "v_exp_f32 v28, v28\n"
    "v_fract_f32 v29, v29\n"
    "v_cos_f32 v42, v29\n"
    "v_sin_f32 v43, v29\n"
    "v_mul_f32 v42, v28, v42\n"
    "v_mul_f32 v43, v28, v43\n"
    "v_mul_f32 v28, 0x3f800000, v16\n"
    "v_mul_f32 v29, 0x3f800000, v17\n"
    "v_exp_f32 v28, v28\n"
    "v_fract_f32 v29, v29\n"
    "v_cos_f32 v40, v29\n"
    "v_sin_f32 v41, v29\n"
    "v_mul_f32 v40, v28, v40\n"
    "v_mul_f32 v41, v28, v41\n"
    "v_mul_f32 v28, 0x43800000, v16\n"
    "v_mul_f32 v29, 0x43800000, v17\n"
    "v_exp_f32 v28, v28\n"
    "v_fract_f32 v29, v29\n"
    "v_cos_f32 v44, v29\n"
    "v_sin_f32 v45, v29\n"
    "v_mul_f32 v44, v28, v44\n"
    "v_mul_f32 v45, v28, v45\n"
    "v_mul_f32 v46, v20, v18\n"
    "v_mul_f32 v47, v20, v19\n"
    "v_fma_f32 v46, -v21, v19, v46\n"
    "v_fma_f32 v47, v21, v18, v47\n"
    "s_waitcnt vmcnt(0)\n"
    "v_lshlrev_b32 v176, 16, v24\n"
    "v_and_b32 v177, 0xffff0000, v24\n"
    "v_lshlrev_b32 v178, 16, v25\n"
    "v_and_b32 v179, 0xffff0000, v25\n"
    "v_lshlrev_b32 v180, 16, v26\n"
    "v_and_b32 v181, 0xffff0000, v26\n"
    "v_lshlrev_b32 v182, 16, v27\n"
    "v_and_b32 v183, 0xffff0000, v27\n"
    "v_mul_f32 v186, v176, v176\n"
    "v_add_f32 v184, v176, v177\n"
    "v_fmac_f32 v186, v177, v177\n"
    "v_fmac_f32 v186, v178, v178\n"
    "v_add_f32 v184, v184, v178\n"
    "v_fmac_f32 v186, v179, v179\n"
    "v_add_f32 v184, v184, v179\n"
    "v_fmac_f32 v186, v180, v180\n"
    "v_add_f32 v184, v184, v180\n"
    "v_fmac_f32 v186, v181, v181\n"
    "v_add_f32 v184, v184, v181\n"
    "v_fmac_f32 v186, v182, v182\n"
    "v_add_f32 v184, v184, v182\n"
    "v_fmac_f32 v186, v183, v183\n"
    "v_add_f32 v184, v184, v183\n"
    "s_nop 1\n"
    "v_add_f32_dpp v184, v184, v184 row_ror:8 row_mask:0xf bank_mask:0xf\n"
    "v_add_f32_dpp v186, v186, v186 row_ror:8 row_mask:0xf bank_mask:0xf\n"
    "s_nop 0\n"
    "v_add_f32_dpp v184, v184, v184 row_ror:4 row_mask:0xf bank_mask:0xf\n"
    "v_add_f32_dpp v186, v186, v186 row_ror:4 row_mask:0xf bank_mask:0xf\n"
    "s_nop 0\n"
    "v_add_f32_dpp v184, v184, v184 row_ror:2 row_mask:0xf bank_mask:0xf\n"
    "v_add_f32_dpp v186, v186, v186 row_ror:2 row_mask:0xf bank_mask:0xf\n"
    "s_nop 0\n"
    "v_add_f32_dpp v184, v184, v184 row_ror:1 row_mask:0xf bank_mask:0xf\n"
    "v_add_f32_dpp v186, v186, v186 row_ror:1 row_mask:0xf bank_mask:0xf\n"
    "v_mov_b32 v176, 1.0\n"
    "s_nop 1\n"
    "v_mfma_f32_16x16x4_f32 v[188:191], v176, v184, 0\n"
    "v_mfma_f32_16x16x4_f32 v[192:195], v176, v186, 0\n"
    "s_nop 8\n"
    "v_mul_f32 v188, 0x3b000000, v188\n"
    "v_mul_f32 v192, 0x3b000000, v192\n"
    "v_fma_f32 v192, -v188, v188, v192\n"
    "v_cvt_pk_bf16_f32 v178, v188, v188\n"
    "v_max_f32 v192, 0, v192\n"
    "v_and_b32 v178, 0xffff0000, v178\n"
    "v_cvt_pk_bf16_f32 v180, v192, v192\n"
    "v_and_b32 v180, 0xffff0000, v180\n"
    "v_add_f32 v180, 0x3727c5ac, v180\n"
    "v_cvt_pk_bf16_f32 v180, v180, v180\n"
    "v_and_b32 v180, 0xffff0000, v180\n"
    "v_sqrt_f32 v180, v180\n"
    "s_nop 0\n"
    "v_cvt_pk_bf16_f32 v179, v180, v180\n"
    "v_and_b32 v179, 0xffff0000, v179\n"
    "v_rcp_f32 v180, v179\n"
    "s_abs_i32 s89, s76\n"
    "s_lshl_b32 s90, s83, 3\n"
    "s_add_u32 s90, s90, 158864\n"
    "s_cmp_lt_u32 s15, s89\n"
    "s_cselect_b64 exec, 1, 0\n"
    "v_mul_f32 v181, -v178, v180\n"
    "v_mov_b32 v178, s90\n"
    "ds_write_b64 v178, v[180:181]\n"
    "s_mov_b64 exec, -1\n"
    "ds_inc_u32 v2, v2\n"
    "s_mul_i32 s89, s12, 43\n"
    "s_mul_i32 s90, s15, 11\n"
    "v_min_u32 v11, 10, v12\n"
    "v_add_u32 v11, s90, v11\n"
    "v_min_u32 v11, 42, v11\n"
    "v_add_u32 v11, s89, v11\n"
    "v_lshlrev_b32 v11, 7, v11\n"
    "s_lshl2_add_u32 s89, s12, s15\n"
    "s_lshl_b32 s89, s89, 9\n"
    "v_and_b32 v10, 3, v12\n"
    "v_lshlrev_b32 v10, 7, v10\n"
    "buffer_load_dword v228, v11, s[32:35], 0 offen\n"
    "v_mov_b32 v74, 0x3f80\n"
    "ds_write_b32 v30, v74\n"
    "v_cvt_pk_bf16_f32 v74, v42, -v43\n"
    "ds_write_b32 v30, v74 offset:272\n"
    "v_mul_f32 v52, v42, v42\n"
    "v_mul_f32 v53, v42, v43\n"
    "v_mul_f32 v54, v40, v40\n"
    "v_mul_f32 v55, v40, v41\n"
    "v_fma_f32 v48, -v43, v43, v52\n"
    "v_fma_f32 v49, v43, v42, v53\n"
    "v_fma_f32 v56, -v41, v41, v54\n"
    "v_fma_f32 v57, v41, v40, v55\n"
    "v_cvt_pk_bf16_f32 v75, v48, -v49\n"
    "ds_write_b32 v30, v75 offset:544\n"
    "v_mul_f32 v52, v48, v42\n"
    "v_mul_f32 v53, v48, v43\n"
    "v_mul_f32 v54, v56, v56\n"
    "v_mul_f32 v55, v56, v57\n"
    "v_fma_f32 v48, -v49, v43, v52\n"
    "v_fma_f32 v49, v49, v42, v53\n"
    "v_fma_f32 v58, -v57, v57, v54\n"
    "v_fma_f32 v59, v57, v56, v55\n"
    "v_cvt_pk_bf16_f32 v76, v48, -v49\n"
    "ds_write_b32 v30, v76 offset:816\n"
    "v_mul_f32 v52, v48, v42\n"
    "v_mul_f32 v53, v48, v43\n"
    "v_mul_f32 v54, v58, v56\n"
    "v_mul_f32 v55, v58, v57\n"
    "v_fma_f32 v48, -v49, v43, v52\n"
    "v_fma_f32 v49, v49, v42, v53\n"
    "v_fma_f32 v60, -v59, v57, v54\n"
    "v_fma_f32 v61, v59, v56, v55\n"
    "v_cvt_pk_bf16_f32 v77, v48, -v49\n"
    "ds_write_b32 v30, v77 offset:1088\n"
    "v_mul_f32 v52, v48, v42\n"
    "v_mul_f32 v53, v48, v43\n"
    "v_mul_f32 v54, v60, v56\n"
    "v_mul_f32 v55, v60, v57\n"
    "v_fma_f32 v48, -v49, v43, v52\n"
    "v_fma_f32 v49, v49, v42, v53\n"
    "v_fma_f32 v62, -v61, v57, v54\n"
    "v_fma_f32 v63, v61, v56, v55\n"
    "v_cvt_pk_bf16_f32 v74, v48, -v49\n"
    "ds_write_b32 v30, v74 offset:1360\n"
    "v_mul_f32 v52, v48, v42\n"
    "v_mul_f32 v53, v48, v43\n"
    "v_mul_f32 v54, v62, v56\n"
    "v_mul_f32 v55, v62, v57\n"
    "v_fma_f32 v48, -v49, v43, v52\n"
    "v_fma_f32 v49, v49, v42, v53\n"
    "v_fma_f32 v64, -v63, v57, v54\n"
    "v_fma_f32 v65, v63, v56, v55\n"
    "v_cvt_pk_bf16_f32 v75, v48, -v49\n"
    "ds_write_b32 v30, v75 offset:1632\n"
    "v_mul_f32 v52, v48, v42\n"
    "v_mul_f32 v53, v48, v43\n"
    "v_mul_f32 v54, v64, v56\n"
    "v_mul_f32 v55, v64, v57\n"
    "v_fma_f32 v48, -v49, v43, v52\n"
    "v_fma_f32 v49, v49, v42, v53\n"
    "v_fma_f32 v66, -v65, v57, v54\n"
    "v_fma_f32 v67, v65, v56, v55\n"
    "v_cvt_pk_bf16_f32 v76, v48, -v49\n"
    "ds_write_b32 v30, v76 offset:1904\n"
    "v_mul_f32 v52, v48, v42\n"
    "v_mul_f32 v53, v48, v43\n"
    "v_mul_f32 v54, v66, v56\n"
    "v_mul_f32 v55, v66, v57\n"
    "v_fma_f32 v48, -v49, v43, v52\n"
    "v_fma_f32 v49, v49, v42, v53\n"
    "v_fma_f32 v68, -v67, v57, v54\n"
    "v_fma_f32 v69, v67, v56, v55\n"
    "v_cvt_pk_bf16_f32 v77, v48, -v49\n"
    "ds_write_b32 v30, v77 offset:2176\n"
    "s_mul_i32 s89, s12, 43\n"
    "s_mul_i32 s90, s15, 11\n"
    "v_min_u32 v11, 10, v12\n"
    "v_add_u32 v11, s90, v11\n"
    "v_min_u32 v11, 42, v11\n"
    "v_add_u32 v11, s89, v11\n"
    "v_lshlrev_b32 v11, 7, v11\n"
    "s_lshl2_add_u32 s89, s12, s15\n"
    "s_lshl_b32 s89, s89, 9\n"
    "v_and_b32 v10, 3, v12\n"
    "v_lshlrev_b32 v10, 7, v10\n"
    "buffer_load_dword v228, v10, s[28:31], s89 offen\n"
    "v_mul_f32 v52, v48, v42\n"
    "v_mul_f32 v53, v48, v43\n"
    "v_fma_f32 v48, -v49, v43, v52\n"
    "v_fma_f32 v49, v49, v42, v53\n"
    "v_cvt_pk_bf16_f32 v74, v48, -v49\n"
    "ds_write_b32 v30, v74 offset:2448\n"
    "v_mul_f32 v52, v48, v42\n"
    "v_mul_f32 v53, v48, v43\n"
    "v_fma_f32 v48, -v49, v43, v52\n"
    "v_fma_f32 v49, v49, v42, v53\n"
    "v_cvt_pk_bf16_f32 v75, v48, -v49\n"
    "ds_write_b32 v30, v75 offset:2720\n"
    "v_mul_f32 v52, v48, v42\n"
    "v_mul_f32 v53, v48, v43\n"
    "v_fma_f32 v48, -v49, v43, v52\n"
    "v_fma_f32 v49, v49, v42, v53\n"
    "v_cvt_pk_bf16_f32 v76, v48, -v49\n"
    "ds_write_b32 v30, v76 offset:2992\n"
    "v_mul_f32 v52, v48, v42\n"
    "v_mul_f32 v53, v48, v43\n"
    "v_fma_f32 v48, -v49, v43, v52\n"
    "v_fma_f32 v49, v49, v42, v53\n"
    "v_cvt_pk_bf16_f32 v77, v48, -v49\n"
    "ds_write_b32 v30, v77 offset:3264\n"
    "v_mul_f32 v52, v48, v42\n"
    "v_mul_f32 v53, v48, v43\n"
    "v_fma_f32 v48, -v49, v43, v52\n"
    "v_fma_f32 v49, v49, v42, v53\n"
    "v_cvt_pk_bf16_f32 v74, v48, -v49\n"
    "ds_write_b32 v30, v74 offset:3536\n"
    "v_mul_f32 v52, v48, v42\n"
    "v_mul_f32 v53, v48, v43\n"
    "v_fma_f32 v48, -v49, v43, v52\n"
    "v_fma_f32 v49, v49, v42, v53\n"
    "v_cvt_pk_bf16_f32 v75, v48, -v49\n"
    "ds_write_b32 v30, v75 offset:3808\n"
    "v_mul_f32 v52, v48, v42\n"
    "v_mul_f32 v53, v48, v43\n"
    "v_fma_f32 v48, -v49, v43, v52\n"
    "v_fma_f32 v49, v49, v42, v53\n"
    "v_cvt_pk_bf16_f32 v76, v48, -v49\n"
    "ds_write_b32 v30, v76 offset:4080\n"
    "ds_read_b128 v[80:83], v32 offset:0\n"
    "ds_read_b128 v[84:87], v32 offset:64\n"
    "ds_read_b128 v[88:91], v32 offset:128\n"
    "ds_read_b128 v[92:95], v32 offset:192\n"
    "s_mul_i32 s89, s12, 43\n"
    "s_mul_i32 s90, s15, 11\n"
    "v_min_u32 v11, 10, v12\n"
    "v_add_u32 v11, s90, v11\n"
    "v_min_u32 v11, 42, v11\n"
    "v_add_u32 v11, s89, v11\n"
    "v_lshlrev_b32 v11, 7, v11\n"
    "s_lshl2_add_u32 s89, s12, s15\n"
    "s_lshl_b32 s89, s89, 9\n"
    "v_and_b32 v10, 3, v12\n"
    "v_lshlrev_b32 v10, 7, v10\n"
    "buffer_load_dword v228, v11, s[36:39], 0 offen\n"
    "v_cvt_pk_bf16_f32 v77, v46, v47\n"
    "ds_write_b32 v30, v77 offset:0\n"
    "v_mul_f32 v52, v46, v40\n"
    "v_mul_f32 v53, v46, v41\n"
    "v_mul_f32 v54, v46, v44\n"
    "v_mul_f32 v55, v46, v45\n"
    "v_fma_f32 v50, -v47, v41, v52\n"
    "v_fma_f32 v51, v47, v40, v53\n"
    "v_fma_f32 v96, -v47, v45, v54\n"
    "v_fma_f32 v97, v47, v44, v55\n"
    "v_cvt_pk_bf16_f32 v74, v50, v51\n"
    "ds_write_b32 v30, v74 offset:272\n"
    "v_mul_f32 v52, v50, v40\n"
    "v_mul_f32 v53, v50, v41\n"
    "v_mul_f32 v54, v96, v44\n"
    "v_mul_f32 v55, v96, v45\n"
    "v_fma_f32 v50, -v51, v41, v52\n"
    "v_fma_f32 v51, v51, v40, v53\n"
    "v_fma_f32 v98, -v97, v45, v54\n"
    "v_fma_f32 v99, v97, v44, v55\n"
    "v_cvt_pk_bf16_f32 v75, v50, v51\n"
    "ds_write_b32 v30, v75 offset:544\n"
    "v_mul_f32 v52, v50, v40\n"
    "v_mul_f32 v53, v50, v41\n"
    "v_mul_f32 v54, v98, v44\n"
    "v_mul_f32 v55, v98, v45\n"
    "v_fma_f32 v50, -v51, v41, v52\n"
    "v_fma_f32 v51, v51, v40, v53\n"
    "v_fma_f32 v100, -v99, v45, v54\n"
    "v_fma_f32 v101, v99, v44, v55\n"
    "v_cvt_pk_bf16_f32 v76, v50, v51\n"
    "ds_write_b32 v30, v76 offset:816\n"
    "v_mul_f32 v52, v50, v40\n"
    "v_mul_f32 v53, v50, v41\n"
    "v_mul_f32 v54, v100, v44\n"
    "v_mul_f32 v55, v100, v45\n"
    "v_fma_f32 v50, -v51, v41, v52\n"
    "v_fma_f32 v51, v51, v40, v53\n"
    "v_fma_f32 v102, -v101, v45, v54\n"
    "v_fma_f32 v103, v101, v44, v55\n"
    "v_cvt_pk_bf16_f32 v77, v50, v51\n"
    "ds_write_b32 v30, v77 offset:1088\n"
    "v_mul_f32 v52, v50, v40\n"
    "v_mul_f32 v53, v50, v41\n"
    "v_mul_f32 v54, v102, v44\n"
    "v_mul_f32 v55, v102, v45\n"
    "v_fma_f32 v50, -v51, v41, v52\n"
    "v_fma_f32 v51, v51, v40, v53\n"
    "v_fma_f32 v104, -v103, v45, v54\n"
    "v_fma_f32 v105, v103, v44, v55\n"
    "v_cvt_pk_bf16_f32 v74, v50, v51\n"
    "ds_write_b32 v30, v74 offset:1360\n"
    "ds_read_b32 v15, v1 offset:0\n"
    "v_mul_f32 v52, v50, v40\n"
    "v_mul_f32 v53, v50, v41\n"
    "v_mul_f32 v54, v104, v44\n"
    "v_mul_f32 v55, v104, v45\n"
    "v_fma_f32 v50, -v51, v41, v52\n"
    "v_fma_f32 v51, v51, v40, v53\n"
    "v_fma_f32 v106, -v105, v45, v54\n"
    "v_fma_f32 v107, v105, v44, v55\n"
    "v_cvt_pk_bf16_f32 v75, v50, v51\n"
    "ds_write_b32 v30, v75 offset:1632\n"
    "v_mul_f32 v52, v50, v40\n"
    "v_mul_f32 v53, v50, v41\n"
    "v_mul_f32 v54, v106, v44\n"
    "v_mul_f32 v55, v106, v45\n"
    "v_fma_f32 v50, -v51, v41, v52\n"
    "v_fma_f32 v51, v51, v40, v53\n"
    "v_fma_f32 v108, -v107, v45, v54\n"
    "v_fma_f32 v109, v107, v44, v55\n"
    "v_cvt_pk_bf16_f32 v76, v50, v51\n"
    "ds_write_b32 v30, v76 offset:1904\n"
    "v_mul_f32 v52, v50, v40\n"
    "v_mul_f32 v53, v50, v41\n"
    "v_fma_f32 v50, -v51, v41, v52\n"
    "v_fma_f32 v51, v51, v40, v53\n"
    "v_cvt_pk_bf16_f32 v77, v50, v51\n"
    "ds_write_b32 v30, v77 offset:2176\n"
    "v_mul_f32 v52, v50, v40\n"
    "v_mul_f32 v53, v50, v41\n"
    "v_fma_f32 v50, -v51, v41, v52\n"
    "v_fma_f32 v51, v51, v40, v53\n"
    "v_cvt_pk_bf16_f32 v74, v50, v51\n"
    "ds_write_b32 v30, v74 offset:2448\n"
    "s_waitcnt lgkmcnt(4)\n"
    "v_cmp_gt_u32 vcc, s72, v15\n"
    "s_cbranch_vccnz .Ls4_ck59\n"
    "s_swappc_b64 s[70:71], s[68:69]\n"
    ".Ls4_ck59:\n"
    "ds_read_b32 v15, v1 offset:0\n"
    "v_mul_f32 v52, v50, v40\n"
    "v_mul_f32 v53, v50, v41\n"
    "v_fma_f32 v50, -v51, v41, v52\n"
    "v_fma_f32 v51, v51, v40, v53\n"
    "v_cvt_pk_bf16_f32 v75, v50, v51\n"
    "ds_write_b32 v30, v75 offset:2720\n"
    "v_mul_f32 v52, v50, v40\n"
    "v_mul_f32 v53, v50, v41\n"
    "v_fma_f32 v50, -v51, v41, v52\n"
    "v_fma_f32 v51, v51, v40, v53\n"
    "v_cvt_pk_bf16_f32 v76, v50, v51\n"
    "ds_write_b32 v30, v76 offset:2992\n"
    "v_mul_f32 v52, v50, v40\n"
    "v_mul_f32 v53, v50, v41\n"
    "v_fma_f32 v50, -v51, v41, v52\n"
    "v_fma_f32 v51, v51, v40, v53\n"
    "v_cvt_pk_bf16_f32 v77, v50, v51\n"
    "ds_write_b32 v30, v77 offset:3264\n"
    "v_mul_f32 v52, v50, v40\n"
    "v_mul_f32 v53, v50, v41\n"
    "v_fma_f32 v50, -v51, v41, v52\n"
    "v_fma_f32 v51, v51, v40, v53\n"
    "v_cvt_pk_bf16_f32 v74, v50, v51\n"
    "ds_write_b32 v30, v74 offset:3536\n"
    "s_waitcnt lgkmcnt(4)\n"
    "v_cmp_gt_u32 vcc, s72, v15\n"
    "s_cbranch_vccnz .Ls4_ck60\n"
    "s_swappc_b64 s[70:71], s[68:69]\n"
    ".Ls4_ck60:\n"
    "ds_read_b32 v15, v1 offset:0\n"
    "v_mul_f32 v52, v50, v40\n"
    "v_mul_f32 v53, v50, v41\n"
    "v_fma_f32 v50, -v51, v41, v52\n"
    "v_fma_f32 v51, v51, v40, v53\n"
    "v_cvt_pk_bf16_f32 v75, v50, v51\n"
    "ds_write_b32 v30, v75 offset:3808\n"
    "v_mul_f32 v52, v50, v40\n"
    "v_mul_f32 v53, v50, v41\n"
    "v_fma_f32 v50, -v51, v41, v52\n"
    "v_fma_f32 v51, v51, v40, v53\n"
    "v_cvt_pk_bf16_f32 v76, v50, v51\n"
    "ds_write_b32 v30, v76 offset:4080\n"
    "ds_read_b128 v[144:147], v32 offset:0\n"
    "ds_read_b128 v[148:151], v32 offset:64\n"
    "ds_read_b128 v[152:155], v32 offset:128\n"
    "ds_read_b128 v[156:159], v32 offset:192\n"
    "s_lshr_b32 s89, s82, 1\n"
    "s_lshl_b32 s89, s89, 14\n"
    "s_and_b32 s90, s82, 1\n"
    "s_lshl2_add_u32 s89, s90, s89\n"
    "s_lshl_b32 s90, s15, 12\n"
    "s_add_u32 s89, s89, s90\n"
    "s_add_u32 s60, s89, 1048576\n"
    "s_lshl2_add_u32 s61, s14, s15\n"
    "s_lshl_b32 s61, s61, 10\n"
    "s_add_u32 s64, s61, 98304\n"
    "s_add_u32 s61, s61, 65536\n"
    "s_lshl1_add_u32 s62, s80, s81\n"
    "s_lshl_b32 s62, s62, 11\n"
    "s_add_u32 s62, s62, 7340032\n"
    "s_lshl_b32 s63, s82, 7\n"
    "s_add_u32 s63, s63, 98304\n"
    "s_waitcnt lgkmcnt(0)\n"
    "v_mfma_f32_16x16x32_bf16 v[112:115], v[144:147], v[80:83], 0\n"
    "v_mfma_f32_16x16x32_bf16 v[112:115], v[148:151], v[84:87], v[112:115]\n"
    "v_mfma_f32_16x16x32_bf16 v[112:115], v[152:155], v[88:91], v[112:115]\n"
    "v_mfma_f32_16x16x32_bf16 v[112:115], v[156:159], v[92:95], v[112:115]\n"
    "v_cvt_pk_bf16_f32 v77, v96, v97\n"
    "ds_write_b32 v30, v77 offset:0\n"
    "v_mul_f32 v52, v96, v56\n"
    "v_mul_f32 v53, v96, v57\n"
    "v_mul_f32 v54, v96, v58\n"
    "v_mul_f32 v55, v96, v59\n"
    "v_fma_f32 v50, -v97, v57, v52\n"
    "v_fma_f32 v51, v97, v56, v53\n"
    "v_fma_f32 v48, -v97, v59, v54\n"
    "v_fma_f32 v49, v97, v58, v55\n"
    "v_cvt_pk_bf16_f32 v74, v50, v51\n"
    "ds_write_b32 v30, v74 offset:272\n"
    "v_cvt_pk_bf16_f32 v75, v48, v49\n"
    "ds_write_b32 v30, v75 offset:544\n"
    "v_cmp_gt_u32 vcc, s72, v15\n"
    "s_cbranch_vccnz .Ls4_ck61\n"
    "s_swappc_b64 s[70:71], s[68:69]\n"
    ".Ls4_ck61:\n"
    "ds_read_b32 v15, v1 offset:0\n"
    "v_mul_f32 v52, v96, v60\n"
    "v_mul_f32 v53, v96, v61\n"
    "v_mul_f32 v54, v96, v62\n"
    "v_mul_f32 v55, v96, v63\n"
    "v_fma_f32 v50, -v97, v61, v52\n"
    "v_fma_f32 v51, v97, v60, v53\n"
    "v_fma_f32 v48, -v97, v63, v54\n"
    "v_fma_f32 v49, v97, v62, v55\n"
    "v_cvt_pk_bf16_f32 v76, v50, v51\n"
    "ds_write_b32 v30, v76 offset:816\n"
    "v_cvt_pk_bf16_f32 v77, v48, v49\n"
    "ds_write_b32 v30, v77 offset:1088\n"
    "v_mul_f32 v52, v96, v64\n"
    "v_mul_f32 v53, v96, v65\n"
    "v_mul_f32 v54, v96, v66\n"
    "v_mul_f32 v55, v96, v67\n"
    "v_fma_f32 v50, -v97, v65, v52\n"
    "v_fma_f32 v51, v97, v64, v53\n"
    "v_fma_f32 v48, -v97, v67, v54\n"
    "v_fma_f32 v49, v97, v66, v55\n"
    "v_cvt_pk_bf16_f32 v74, v50, v51\n"
    "ds_write_b32 v30, v74 offset:1360\n"
    "v_cvt_pk_bf16_f32 v75, v48, v49\n"
    "ds_write_b32 v30, v75 offset:1632\n"
    "s_waitcnt lgkmcnt(4)\n"
    "v_cmp_gt_u32 vcc, s72, v15\n"
    "s_cbranch_vccnz .Ls4_ck62\n"
    "s_swappc_b64 s[70:71], s[68:69]\n"
    ".Ls4_ck62:\n"
    "ds_read_b32 v15, v1 offset:0\n"
    "v_mul_f32 v52, v96, v68\n"
    "v_mul_f32 v53, v96, v69\n"
    "v_mul_f32 v54, v96, v42\n"
    "v_mul_f32 v55, v96, v43\n"
    "v_fma_f32 v50, -v97, v69, v52\n"
    "v_fma_f32 v51, v97, v68, v53\n"
    "v_fma_f32 v48, -v97, v43, v54\n"
    "v_fma_f32 v49, v97, v42, v55\n"
    "v_cvt_pk_bf16_f32 v76, v50, v51\n"
    "ds_write_b32 v30, v76 offset:1904\n"
    "v_cvt_pk_bf16_f32 v77, v48, v49\n"
    "ds_write_b32 v30, v77 offset:2176\n"
    "v_cvt_pk_bf16_f32 v74, v98, v99\n"
    "ds_write_b32 v30, v74 offset:2448\n"
    "v_mul_f32 v52, v98, v58\n"
    "v_mul_f32 v53, v98, v59\n"
    "v_mul_f32 v54, v98, v62\n"
    "v_mul_f32 v55, v98, v63\n"
    "v_fma_f32 v50, -v99, v59, v52\n"
    "v_fma_f32 v51, v99, v58, v53\n"
    "v_fma_f32 v48, -v99, v63, v54\n"
    "v_fma_f32 v49, v99, v62, v55\n"
    "s_waitcnt lgkmcnt(3)\n"
    "v_cmp_gt_u32 vcc, s72, v15\n"
    "s_cbranch_vccnz .Ls4_ck63\n"
    "s_swappc_b64 s[70:71], s[68:69]\n"
    ".Ls4_ck63:\n"
    "ds_read_b32 v15, v1 offset:0\n"
    "v_cvt_pk_bf16_f32 v75, v50, v51\n"
    "ds_write_b32 v30, v75 offset:2720\n"
    "v_cvt_pk_bf16_f32 v76, v48, v49\n"
    "ds_write_b32 v30, v76 offset:2992\n"
    "v_mul_f32 v52, v98, v66\n"
    "v_mul_f32 v53, v98, v67\n"
    "v_mul_f32 v54, v98, v42\n"
    "v_mul_f32 v55, v98, v43\n"
    "v_fma_f32 v50, -v99, v67, v52\n"
    "v_fma_f32 v51, v99, v66, v53\n"
    "v_fma_f32 v48, -v99, v43, v54\n"
    "v_fma_f32 v49, v99, v42, v55\n"
    "v_cvt_pk_bf16_f32 v77, v50, v51\n"
    "ds_write_b32 v30, v77 offset:3264\n"
    "v_cvt_pk_bf16_f32 v74, v48, v49\n"
    "ds_write_b32 v30, v74 offset:3536\n"
    "v_cvt_pk_bf16_f32 v75, v102, v103\n"
    "ds_write_b32 v30, v75 offset:3808\n"
    "s_waitcnt lgkmcnt(5)\n"
    "v_cmp_gt_u32 vcc, s72, v15\n"
    "s_cbranch_vccnz .Ls4_ck64\n"
    "s_swappc_b64 s[70:71], s[68:69]\n"
    ".Ls4_ck64:\n"
    "ds_read_b32 v15, v1 offset:0\n"
    "v_mul_f32 v52, v102, v62\n"
    "v_mul_f32 v53, v102, v63\n"
    "v_mul_f32 v54, v102, v42\n"
    "v_mul_f32 v55, v102, v43\n"
    "v_fma_f32 v50, -v103, v63, v52\n"
    "v_fma_f32 v51, v103, v62, v53\n"
    "v_fma_f32 v48, -v103, v43, v54\n"
    "v_fma_f32 v49, v103, v42, v55\n"
    "v_cvt_pk_bf16_f32 v76, v50, v51\n"
    "ds_write_b32 v30, v76 offset:4080\n"
    "v_cvt_pk_bf16_f32 v77, v48, v49\n"
    "ds_write_b32 v30, v77 offset:4352\n"
    "ds_read_b128 v[144:147], v33 offset:0\n"
    "ds_read_b128 v[148:151], v33 offset:64\n"
    "ds_read_b128 v[152:155], v33 offset:128\n"
    "ds_read_b128 v[156:159], v33 offset:192\n"
    "ds_read_b128 v[160:163], v34 offset:2448\n"
    "ds_read_b128 v[164:167], v34 offset:2512\n"
    "ds_read_b128 v[168:171], v34 offset:2576\n"
    "ds_read_b128 v[172:175], v34 offset:2640\n"
    "ds_read_b128 v[176:179], v35 offset:3808\n"
    "ds_read_b128 v[180:183], v35 offset:3872\n"
    "ds_read_b128 v[184:187], v35 offset:3936\n"
    "ds_read_b128 v[188:191], v35 offset:4000\n"
    "v_cvt_pk_bf16_f32 v74, v100, v101\n"
    "ds_write_b32 v30, v74 offset:0\n"
    "s_waitcnt lgkmcnt(15)\n"
    "v_cmp_gt_u32 vcc, s72, v15\n"
    "s_cbranch_vccnz .Ls4_ck65\n"
    "s_swappc_b64 s[70:71], s[68:69]\n"
    ".Ls4_ck65:\n"
    "ds_read_b32 v15, v1 offset:0\n"
    "v_mul_f32 v52, v100, v58\n"
    "v_mul_f32 v53, v100, v59\n"
    "v_mul_f32 v54, v100, v62\n"
    "v_mul_f32 v55, v100, v63\n"
    "v_fma_f32 v50, -v101, v59, v52\n"
    "v_fma_f32 v51, v101, v58, v53\n"
    "v_fma_f32 v48, -v101, v63, v54\n"
    "v_fma_f32 v49, v101, v62, v55\n"
    "v_cvt_pk_bf16_f32 v75, v50, v51\n"
    "ds_write_b32 v30, v75 offset:272\n"
    "v_cvt_pk_bf16_f32 v76, v48, v49\n"
    "ds_write_b32 v30, v76 offset:544\n"
    "v_mul_f32 v52, v100, v66\n"
    "v_mul_f32 v53, v100, v67\n"
    "v_mul_f32 v54, v100, v42\n"
    "v_mul_f32 v55, v100, v43\n"
    "v_fma_f32 v50, -v101, v67, v52\n"
    "v_fma_f32 v51, v101, v66, v53\n"
    "v_fma_f32 v48, -v101, v43, v54\n"
    "v_fma_f32 v49, v101, v42, v55\n"
    "v_cvt_pk_bf16_f32 v77, v50, v51\n"
    "ds_write_b32 v30, v77 offset:816\n"
    "v_cvt_pk_bf16_f32 v74, v48, v49\n"
    "ds_write_b32 v30, v74 offset:1088\n"
    "s_waitcnt lgkmcnt(4)\n"
    "v_cmp_gt_u32 vcc, s72, v15\n"
    "s_cbranch_vccnz .Ls4_ck66\n"
    "s_swappc_b64 s[70:71], s[68:69]\n"
    ".Ls4_ck66:\n"
    "ds_read_b32 v15, v1 offset:0\n"
    "v_mfma_f32_16x16x32_bf16 v[116:119], v[144:147], v[80:83], 0\n"
    "v_mfma_f32_16x16x32_bf16 v[116:119], v[148:151], v[84:87], v[116:119]\n"
    "v_mfma_f32_16x16x32_bf16 v[116:119], v[152:155], v[88:91], v[116:119]\n"
    "v_mfma_f32_16x16x32_bf16 v[116:119], v[156:159], v[92:95], v[116:119]\n"
    "v_mfma_f32_16x16x32_bf16 v[120:123], v[160:163], v[80:83], 0\n"
    "v_mfma_f32_16x16x32_bf16 v[120:123], v[164:167], v[84:87], v[120:123]\n"
    "v_mfma_f32_16x16x32_bf16 v[120:123], v[168:171], v[88:91], v[120:123]\n"
    "v_mfma_f32_16x16x32_bf16 v[120:123], v[172:175], v[92:95], v[120:123]\n"
    "v_mfma_f32_16x16x32_bf16 v[128:131], v[176:179], v[80:83], 0\n"
    "v_mfma_f32_16x16x32_bf16 v[128:131], v[180:183], v[84:87], v[128:131]\n"
    "v_mfma_f32_16x16x32_bf16 v[128:131], v[184:187], v[88:91], v[128:131]\n"
    "v_mfma_f32_16x16x32_bf16 v[128:131], v[188:191], v[92:95], v[128:131]\n"
    "v_cvt_pk_bf16_f32 v75, v104, v105\n"
    "ds_write_b32 v30, v75 offset:1360\n"
    "v_mul_f32 v52, v104, v62\n"
    "v_mul_f32 v53, v104, v63\n"
    "v_mul_f32 v54, v104, v42\n"
    "v_mul_f32 v55, v104, v43\n"
    "v_fma_f32 v50, -v105, v63, v52\n"
    "v_fma_f32 v51, v105, v62, v53\n"
    "v_fma_f32 v48, -v105, v43, v54\n"
    "v_fma_f32 v49, v105, v42, v55\n"
    "v_cvt_pk_bf16_f32 v76, v50, v51\n"
    "ds_write_b32 v30, v76 offset:1632\n"
    "v_cvt_pk_bf16_f32 v77, v48, v49\n"
    "ds_write_b32 v30, v77 offset:1904\n"
    "s_waitcnt lgkmcnt(3)\n"
    "v_cmp_gt_u32 vcc, s72, v15\n"
    "s_cbranch_vccnz .Ls4_ck67\n"
    "s_swappc_b64 s[70:71], s[68:69]\n"
    ".Ls4_ck67:\n"
    "ds_read_b32 v15, v1 offset:0\n"
    "v_cvt_pk_bf16_f32 v74, v106, v107\n"
    "ds_write_b32 v30, v74 offset:2176\n"
    "v_mul_f32 v52, v106, v62\n"
    "v_mul_f32 v53, v106, v63\n"
    "v_mul_f32 v54, v106, v42\n"
    "v_mul_f32 v55, v106, v43\n"
    "v_fma_f32 v50, -v107, v63, v52\n"
    "v_fma_f32 v51, v107, v62, v53\n"
    "v_fma_f32 v48, -v107, v43, v54\n"
    "v_fma_f32 v49, v107, v42, v55\n"
    "v_cvt_pk_bf16_f32 v75, v50, v51\n"
    "ds_write_b32 v30, v75 offset:2448\n"
    "v_cvt_pk_bf16_f32 v76, v48, v49\n"
    "ds_write_b32 v30, v76 offset:2720\n"
    "v_cvt_pk_bf16_f32 v77, v108, v109\n"
    "ds_write_b32 v30, v77 offset:2992\n"
    "s_waitcnt lgkmcnt(4)\n"
    "v_cmp_gt_u32 vcc, s72, v15\n"
    "s_cbranch_vccnz .Ls4_ck68\n"
    "s_swappc_b64 s[70:71], s[68:69]\n"
    ".Ls4_ck68:\n"
    "ds_read_b32 v15, v1 offset:0\n"
    "v_mul_f32 v52, v108, v62\n"
    "v_mul_f32 v53, v108, v63\n"
    "v_mul_f32 v54, v108, v42\n"
    "v_mul_f32 v55, v108, v43\n"
    "v_fma_f32 v50, -v109, v63, v52\n"
    "v_fma_f32 v51, v109, v62, v53\n"
    "v_fma_f32 v48, -v109, v43, v54\n"
    "v_fma_f32 v49, v109, v42, v55\n"
    "v_cvt_pk_bf16_f32 v74, v50, v51\n"
    "ds_write_b32 v30, v74 offset:3264\n"
    "v_cvt_pk_bf16_f32 v75, v48, v49\n"
    "ds_write_b32 v30, v75 offset:3536\n"
    "ds_read_b128 v[144:147], v34 offset:0\n"
    "ds_read_b128 v[148:151], v34 offset:64\n"
    "ds_read_b128 v[152:155], v34 offset:128\n"
    "ds_read_b128 v[156:159], v34 offset:192\n"
    "ds_read_b128 v[160:163], v35 offset:1360\n"
    "ds_read_b128 v[164:167], v35 offset:1424\n"
    "ds_read_b128 v[168:171], v35 offset:1488\n"
    "ds_read_b128 v[172:175], v35 offset:1552\n"
    "ds_read_b128 v[176:179], v35 offset:2176\n"
    "ds_read_b128 v[180:183], v35 offset:2240\n"
    "ds_read_b128 v[184:187], v35 offset:2304\n"
    "ds_read_b128 v[188:191], v35 offset:2368\n"
    "ds_read_b128 v[192:195], v35 offset:2992\n"
    "ds_read_b128 v[196:199], v35 offset:3056\n"
    "ds_read_b128 v[200:203], v35 offset:3120\n"
    "ds_read_b128 v[204:207], v35 offset:3184\n"
    "buffer_load_dwordx2 v[6:7], v212, s[24:27], s61 offen sc1\n"
    "s_waitcnt lgkmcnt(15)\n"
    "v_cmp_gt_u32 vcc, s72, v15\n"
    "s_cbranch_vccnz .Ls4_ck69\n"
    "s_swappc_b64 s[70:71], s[68:69]\n"
    ".Ls4_ck69:\n"
    "ds_read_b32 v15, v1 offset:0\n"
    "s_waitcnt lgkmcnt(13)\n"
    "v_mfma_f32_16x16x32_bf16 v[124:127], v[144:147], v[80:83], 0\n"
    "v_mfma_f32_16x16x32_bf16 v[124:127], v[148:151], v[84:87], v[124:127]\n"
    "v_mfma_f32_16x16x32_bf16 v[124:127], v[152:155], v[88:91], v[124:127]\n"
    "v_mfma_f32_16x16x32_bf16 v[124:127], v[156:159], v[92:95], v[124:127]\n"
    "s_waitcnt lgkmcnt(9)\n"
    "v_mfma_f32_16x16x32_bf16 v[132:135], v[160:163], v[80:83], 0\n"
    "v_mfma_f32_16x16x32_bf16 v[132:135], v[164:167], v[84:87], v[132:135]\n"
    "v_mfma_f32_16x16x32_bf16 v[132:135], v[168:171], v[88:91], v[132:135]\n"
    "v_mfma_f32_16x16x32_bf16 v[132:135], v[172:175], v[92:95], v[132:135]\n"
    "s_waitcnt lgkmcnt(5)\n"
    "v_mfma_f32_16x16x32_bf16 v[136:139], v[176:179], v[80:83], 0\n"
    "v_mfma_f32_16x16x32_bf16 v[136:139], v[180:183], v[84:87], v[136:139]\n"
    "v_mfma_f32_16x16x32_bf16 v[136:139], v[184:187], v[88:91], v[136:139]\n"
    "v_mfma_f32_16x16x32_bf16 v[136:139], v[188:191], v[92:95], v[136:139]\n"
    "s_waitcnt lgkmcnt(1)\n"
    "v_mfma_f32_16x16x32_bf16 v[140:143], v[192:195], v[80:83], 0\n"
    "v_mfma_f32_16x16x32_bf16 v[140:143], v[196:199], v[84:87], v[140:143]\n"
    "v_mfma_f32_16x16x32_bf16 v[140:143], v[200:203], v[88:91], v[140:143]\n"
    "v_mfma_f32_16x16x32_bf16 v[140:143], v[204:207], v[92:95], v[140:143]\n"
    "s_waitcnt vmcnt(0)\n"
    "v_cmp_ne_u64 vcc, s[96:97], v[6:7]\n"
    "s_cbranch_vccnz .Ls4_ps70\n"
    ".Ls4_pb71:\n"
    "buffer_load_dwordx3 v[232:234], v0, s[24:27], s60 offen offset:0 sc1\n"
    "buffer_load_dwordx3 v[236:238], v0, s[24:27], s60 offen offset:1024 sc1\n"
    "buffer_load_dwordx3 v[240:242], v0, s[24:27], s60 offen offset:2048 sc1\n"
    "buffer_load_dwordx3 v[244:246], v0, s[24:27], s60 offen offset:3072 sc1\n"
    "s_cmp_eq_u32 s81, 0\n"
    "s_cbranch_scc0 .Ls4_h172\n"
    "ds_write_b128 v208, v[128:131] offset:0\n"
    "ds_write_b128 v208, v[132:135] offset:1024\n"
    "ds_write_b128 v208, v[136:139] offset:2048\n"
    "ds_write_b128 v208, v[140:143] offset:3072\n"
    "ds_inc_u32 v2, v2\n"
    "s_waitcnt lgkmcnt(5)\n"
    "v_cmp_gt_u32 vcc, s72, v15\n"
    "s_cbranch_vccnz .Ls4_ck74\n"
    "s_swappc_b64 s[70:71], s[68:69]\n"
    ".Ls4_ck74:\n"
    "ds_read_b32 v15, v1 offset:0\n"
    "ds_read_b32 v3, v9 offset:0\n"
    "s_waitcnt lgkmcnt(0)\n"
    "v_cmp_gt_u32 vcc, 2, v3\n"
    "s_cbranch_vccnz .Ls4_cs75\n"
    ".Ls4_cb76:\n"
    "ds_read_b128 v[144:147], v209 offset:0\n"
    "ds_read_b128 v[148:151], v209 offset:1024\n"
    "ds_read_b128 v[152:155], v209 offset:2048\n"
    "ds_read_b128 v[156:159], v209 offset:3072\n"
    "ds_inc_u32 v2, v2\n"
    "ds_read_b32 v3, v9 offset:0\n"
    "s_waitcnt lgkmcnt(5)\n"
    "v_add_f32 v144, v144, v112\n"
    "v_add_f32 v145, v145, v113\n"
    "v_add_f32 v146, v146, v114\n"
    "v_add_f32 v147, v147, v115\n"
    "v_cvt_pk_bf16_f32 v176, v144, v145\n"
    "v_cvt_pk_bf16_f32 v177, v146, v147\n"
    "v_alignbit_b32 v178, v177, v176, 16\n"
    "s_waitcnt lgkmcnt(4)\n"
    "v_add_f32 v148, v148, v116\n"
    "v_add_f32 v149, v149, v117\n"
    "v_add_f32 v150, v150, v118\n"
    "v_add_f32 v151, v151, v119\n"
    "v_cvt_pk_bf16_f32 v180, v148, v149\n"
    "v_cvt_pk_bf16_f32 v181, v150, v151\n"
    "v_alignbit_b32 v182, v181, v180, 16\n"
    "s_waitcnt lgkmcnt(3)\n"
    "v_add_f32 v152, v152, v120\n"
    "v_add_f32 v153, v153, v121\n"
    "v_add_f32 v154, v154, v122\n"
    "v_add_f32 v155, v155, v123\n"
    "v_cvt_pk_bf16_f32 v184, v152, v153\n"
    "v_cvt_pk_bf16_f32 v185, v154, v155\n"
    "v_alignbit_b32 v186, v185, v184, 16\n"
    "s_waitcnt lgkmcnt(2)\n"
    "v_add_f32 v156, v156, v124\n"
    "v_add_f32 v157, v157, v125\n"
    "v_add_f32 v158, v158, v126\n"
    "v_add_f32 v159, v159, v127\n"
    "v_cvt_pk_bf16_f32 v188, v156, v157\n"
    "v_cvt_pk_bf16_f32 v189, v158, v159\n"
    "v_alignbit_b32 v190, v189, v188, 16\n"
    "s_branch .Ls4_hj73\n"
    ".Ls4_h172:\n"
    "ds_write_b128 v208, v[112:115] offset:0\n"
    "ds_write_b128 v208, v[116:119] offset:1024\n"
    "ds_write_b128 v208, v[120:123] offset:2048\n"
    "ds_write_b128 v208, v[124:127] offset:3072\n"
    "ds_inc_u32 v2, v2\n"
    "s_waitcnt lgkmcnt(5)\n"
    "v_cmp_gt_u32 vcc, s72, v15\n"
    "s_cbranch_vccnz .Ls4_ck77\n"
    "s_swappc_b64 s[70:71], s[68:69]\n"
    ".Ls4_ck77:\n"
    "ds_read_b32 v15, v1 offset:0\n"
    "ds_read_b32 v3, v9 offset:0\n"
    "s_waitcnt lgkmcnt(0)\n"
    "v_cmp_gt_u32 vcc, 2, v3\n"
    "s_cbranch_vccnz .Ls4_cs78\n"
    ".Ls4_cb79:\n"
    "ds_read_b128 v[144:147], v209 offset:0\n"
    "ds_read_b128 v[148:151], v209 offset:1024\n"
    "ds_read_b128 v[152:155], v209 offset:2048\n"
    "ds_read_b128 v[156:159], v209 offset:3072\n"
    "ds_inc_u32 v2, v2\n"
    "ds_read_b32 v3, v9 offset:0\n"
    "s_waitcnt lgkmcnt(5)\n"
    "v_add_f32 v144, v144, v128\n"
    "v_add_f32 v145, v145, v129\n"
    "v_add_f32 v146, v146, v130\n"
    "v_add_f32 v147, v147, v131\n"
    "v_cvt_pk_bf16_f32 v176, v144, v145\n"
    "v_cvt_pk_bf16_f32 v177, v146, v147\n"
    "v_alignbit_b32 v178, v177, v176, 16\n"
    "s_waitcnt lgkmcnt(4)\n"
    "v_add_f32 v148, v148, v132\n"
    "v_add_f32 v149, v149, v133\n"
    "v_add_f32 v150, v150, v134\n"
    "v_add_f32 v151, v151, v135\n"
    "v_cvt_pk_bf16_f32 v180, v148, v149\n"
    "v_cvt_pk_bf16_f32 v181, v150, v151\n"
    "v_alignbit_b32 v182, v181, v180, 16\n"
    "s_waitcnt lgkmcnt(3)\n"
    "v_add_f32 v152, v152, v136\n"
    "v_add_f32 v153, v153, v137\n"
    "v_add_f32 v154, v154, v138\n"
    "v_add_f32 v155, v155, v139\n"
    "v_cvt_pk_bf16_f32 v184, v152, v153\n"
    "v_cvt_pk_bf16_f32 v185, v154, v155\n"
    "v_alignbit_b32 v186, v185, v184, 16\n"
    "s_waitcnt lgkmcnt(2)\n"
    "v_add_f32 v156, v156, v140\n"
    "v_add_f32 v157, v157, v141\n"
    "v_add_f32 v158, v158, v142\n"
    "v_add_f32 v159, v159, v143\n"
    "v_cvt_pk_bf16_f32 v188, v156, v157\n"
    "v_cvt_pk_bf16_f32 v189, v158, v159\n"
    "v_alignbit_b32 v190, v189, v188, 16\n"
    ".Ls4_hj73:\n"
    "v_mov_b32 v22, 0\n"
    "s_waitcnt lgkmcnt(0)\n"
    "v_cmp_gt_u32 vcc, 3, v3\n"
    "s_cbranch_vccnz .Ls4_cs80\n"
    ".Ls4_cb81:\n"
    "s_cmp_eq_u32 s81, 0\n"
    "s_cselect_b64 exec, 0xff, 0\n"
    "ds_write_b32 v211, v22\n"
    "ds_write_b32 v211, v22 offset:4160\n"
    "s_mov_b64 exec, -1\n"
    "ds_write_b64 v210, v[176:177] offset:32\n"
    "ds_write_b32 v210, v178 offset:4192\n"
    "ds_write_b16 v210, v176 offset:4190\n"
    "ds_write_b16_d16_hi v210, v177 offset:4196\n"
    "ds_write_b64 v210, v[180:181] offset:544\n"
    "ds_write_b32 v210, v182 offset:4704\n"
    "ds_write_b16 v210, v180 offset:4702\n"
    "ds_write_b16_d16_hi v210, v181 offset:4708\n"
    "ds_write_b64 v210, v[184:185] offset:1056\n"
    "ds_write_b32 v210, v186 offset:5216\n"
    "ds_write_b16 v210, v184 offset:5214\n"
    "ds_write_b16_d16_hi v210, v185 offset:5220\n"
    "ds_write_b64 v210, v[188:189] offset:1568\n"
    "ds_write_b32 v210, v190 offset:5728\n"
    "ds_write_b16 v210, v188 offset:5726\n"
    "ds_write_b16_d16_hi v210, v189 offset:5732\n"
    "s_mov_b32 s91, 0x05040100\n"
    "s_mov_b32 s92, 0x07060302\n"
    "v_cmp_gt_u32 vcc, s72, v15\n"
    "s_cbranch_vccnz .Ls4_ck82\n"
    "s_swappc_b64 s[70:71], s[68:69]\n"
    ".Ls4_ck82:\n"
    ".Ls4_bl83:\n"
    "s_cmp_eq_u32 s72, 32767\n"
    "s_cbranch_scc0 .Ls4_bl84\n"
    ".Ls4_bl85:\n"
    "s_mov_b32 exec_hi, 0x0fffffff\n"
    "ds_write_b32 v214, v22\n"
    "s_mov_b32 exec_hi, -1\n"
    "s_waitcnt vmcnt(3)\n"
    "v_perm_b32 v233, v232, v234, s91\n"
    "v_perm_b32 v235, v232, v234, s92\n"
    "ds_write_b32 v213, v233 offset:768\n"
    "ds_write_b32 v213, v235 offset:5376\n"
    "s_waitcnt vmcnt(2)\n"
    "v_perm_b32 v237, v236, v238, s91\n"
    "v_perm_b32 v239, v236, v238, s92\n"
    "ds_write_b32 v213, v237 offset:512\n"
    "ds_write_b32 v213, v239 offset:5120\n"
    "s_waitcnt vmcnt(1)\n"
    "v_perm_b32 v241, v240, v242, s91\n"
    "v_perm_b32 v243, v240, v242, s92\n"
    "ds_write_b32 v213, v241 offset:256\n"
    "ds_write_b32 v213, v243 offset:4864\n"
    "s_waitcnt vmcnt(0)\n"
    "v_perm_b32 v245, v244, v246, s91\n"
    "v_perm_b32 v247, v244, v246, s92\n"
    "ds_write_b32 v213, v245 offset:0\n"
    "ds_write_b32 v213, v247 offset:4608\n"
    "ds_inc_u32 v2, v2\n"
    "ds_read_b32 v3, v1 offset:1024\n"
    "s_waitcnt lgkmcnt(0)\n"
    "v_cmp_gt_u32 vcc, 4, v3\n"
    "s_cbranch_vccnz .Ls4_cs87\n"
    ".Ls4_cb88:\n"
    "ds_read2_b32 v[136:137], v220 offset0:0 offset1:1\n"
    "ds_read2_b32 v[138:139], v220 offset0:2 offset1:3\n"
    "ds_read_b128 v[72:75], v31 offset:3584\n"
    "ds_read_b128 v[76:79], v31 offset:3072\n"
    "ds_read_b128 v[80:83], v31 offset:2560\n"
    "ds_read_b128 v[84:87], v31 offset:2048\n"
    "ds_read_b128 v[88:91], v31 offset:1536\n"
    "ds_read_b128 v[92:95], v31 offset:1024\n"
    "ds_read_b128 v[96:99], v31 offset:512\n"
    "ds_read_b128 v[100:103], v31 offset:0\n"
    "ds_read2_b32 v[140:141], v220 offset0:128 offset1:129\n"
    "ds_read2_b32 v[142:143], v220 offset0:130 offset1:131\n"
    "ds_read2_b32 v[144:145], v221 offset0:0 offset1:1\n"
    "ds_read2_b32 v[146:147], v221 offset0:2 offset1:3\n"
    "ds_read2_b32 v[148:149], v221 offset0:128 offset1:129\n"
    "ds_read2_b32 v[150:151], v221 offset0:130 offset1:131\n"
    "ds_read2_b32 v[152:153], v222 offset0:0 offset1:1\n"
    "ds_read2_b32 v[154:155], v222 offset0:2 offset1:3\n"
    "ds_read2_b32 v[156:157], v222 offset0:128 offset1:129\n"
    "ds_read2_b32 v[158:159], v222 offset0:130 offset1:131\n"
    "ds_read2_b32 v[160:161], v223 offset0:0 offset1:1\n"
    "ds_read2_b32 v[162:163], v223 offset0:2 offset1:3\n"
    "ds_read2_b32 v[164:165], v223 offset0:128 offset1:129\n"
    "ds_read2_b32 v[166:167], v223 offset0:130 offset1:131\n"
    "s_waitcnt lgkmcnt(0)\n"
    "v_mfma_f32_16x16x32_bf16 v[40:43], v[136:139], v[72:75], 0\n"
    "ds_read2_b32 v[168:169], v220 offset0:16 offset1:17\n"
    "v_mfma_f32_16x16x32_bf16 v[44:47], v[136:139], v[76:79], 0\n"
    "ds_read2_b32 v[170:171], v220 offset0:18 offset1:19\n"
    "v_mfma_f32_16x16x32_bf16 v[48:51], v[136:139], v[80:83], 0\n"
    "ds_read_b128 v[104:107], v31 offset:3648\n"
    "v_mfma_f32_16x16x32_bf16 v[52:55], v[136:139], v[84:87], 0\n"
    "ds_read_b128 v[108:111], v31 offset:3136\n"
    "v_mfma_f32_16x16x32_bf16 v[56:59], v[136:139], v[88:91], 0\n"
    "ds_read_b128 v[112:115], v31 offset:2624\n"
    "v_mfma_f32_16x16x32_bf16 v[60:63], v[136:139], v[92:95], 0\n"
    "ds_read_b128 v[116:119], v31 offset:2112\n"
    "v_mfma_f32_16x16x32_bf16 v[64:67], v[136:139], v[96:99], 0\n"
    "ds_read_b128 v[120:123], v31 offset:1600\n"
    "v_mfma_f32_16x16x32_bf16 v[68:71], v[136:139], v[100:103], 0\n"
    "ds_read_b128 v[124:127], v31 offset:1088\n"
    "v_mfma_f32_16x16x32_bf16 v[44:47], v[140:143], v[72:75], v[44:47]\n"
    "ds_read_b128 v[128:131], v31 offset:576\n"
    "v_mfma_f32_16x16x32_bf16 v[48:51], v[140:143], v[76:79], v[48:51]\n"
    "ds_read_b128 v[132:135], v31 offset:64\n"
    "v_mfma_f32_16x16x32_bf16 v[52:55], v[140:143], v[80:83], v[52:55]\n"
    "ds_read2_b32 v[172:173], v220 offset0:144 offset1:145\n"
    "v_mfma_f32_16x16x32_bf16 v[56:59], v[140:143], v[84:87], v[56:59]\n"
    "ds_read2_b32 v[174:175], v220 offset0:146 offset1:147\n"
    "v_mfma_f32_16x16x32_bf16 v[60:63], v[140:143], v[88:91], v[60:63]\n"
    "ds_read2_b32 v[176:177], v221 offset0:16 offset1:17\n"
    "v_mfma_f32_16x16x32_bf16 v[64:67], v[140:143], v[92:95], v[64:67]\n"
    "ds_read2_b32 v[178:179], v221 offset0:18 offset1:19\n"
    "v_mfma_f32_16x16x32_bf16 v[68:71], v[140:143], v[96:99], v[68:71]\n"
    "ds_read2_b32 v[180:181], v221 offset0:144 offset1:145\n"
    "v_mfma_f32_16x16x32_bf16 v[48:51], v[144:147], v[72:75], v[48:51]\n"
    "ds_read2_b32 v[182:183], v221 offset0:146 offset1:147\n"
    "v_mfma_f32_16x16x32_bf16 v[52:55], v[144:147], v[76:79], v[52:55]\n"
    "ds_read2_b32 v[184:185], v222 offset0:16 offset1:17\n"
    "v_mfma_f32_16x16x32_bf16 v[56:59], v[144:147], v[80:83], v[56:59]\n"
    "ds_read2_b32 v[186:187], v222 offset0:18 offset1:19\n"
    "v_mfma_f32_16x16x32_bf16 v[60:63], v[144:147], v[84:87], v[60:63]\n"
    "ds_read2_b32 v[188:189], v222 offset0:144 offset1:145\n"
    "v_mfma_f32_16x16x32_bf16 v[64:67], v[144:147], v[88:91], v[64:67]\n"
    "ds_read2_b32 v[190:191], v222 offset0:146 offset1:147\n"
    "v_mfma_f32_16x16x32_bf16 v[68:71], v[144:147], v[92:95], v[68:71]\n"
    "ds_read2_b32 v[192:193], v223 offset0:16 offset1:17\n"
    "v_mfma_f32_16x16x32_bf16 v[52:55], v[148:151], v[72:75], v[52:55]\n"
    "ds_read2_b32 v[194:195], v223 offset0:18 offset1:19\n"
    "v_mfma_f32_16x16x32_bf16 v[56:59], v[148:151], v[76:79], v[56:59]\n"
    "ds_read2_b32 v[196:197], v223 offset0:144 offset1:145\n"
    "v_mfma_f32_16x16x32_bf16 v[60:63], v[148:151], v[80:83], v[60:63]\n"
    "ds_read2_b32 v[198:199], v223 offset0:146 offset1:147\n"
    "v_mfma_f32_16x16x32_bf16 v[64:67], v[148:151], v[84:87], v[64:67]\n"
    "v_mfma_f32_16x16x32_bf16 v[68:71], v[148:151], v[88:91], v[68:71]\n"
    "v_mfma_f32_16x16x32_bf16 v[56:59], v[152:155], v[72:75], v[56:59]\n"
    "v_mfma_f32_16x16x32_bf16 v[60:63], v[152:155], v[76:79], v[60:63]\n"
    "v_mfma_f32_16x16x32_bf16 v[64:67], v[152:155], v[80:83], v[64:67]\n"
    "v_mfma_f32_16x16x32_bf16 v[68:71], v[152:155], v[84:87], v[68:71]\n"
    "v_mfma_f32_16x16x32_bf16 v[60:63], v[156:159], v[72:75], v[60:63]\n"
    "v_mfma_f32_16x16x32_bf16 v[64:67], v[156:159], v[76:79], v[64:67]\n"
    "v_mfma_f32_16x16x32_bf16 v[68:71], v[156:159], v[80:83], v[68:71]\n"
    "v_mfma_f32_16x16x32_bf16 v[64:67], v[160:163], v[72:75], v[64:67]\n"
    "v_mfma_f32_16x16x32_bf16 v[68:71], v[160:163], v[76:79], v[68:71]\n"
    "v_mfma_f32_16x16x32_bf16 v[68:71], v[164:167], v[72:75], v[68:71]\n"
    "s_waitcnt lgkmcnt(0)\n"
    "v_mfma_f32_16x16x32_bf16 v[40:43], v[168:171], v[104:107], v[40:43]\n"
    "ds_read2_b32 v[136:137], v220 offset0:32 offset1:33\n"
    "v_mfma_f32_16x16x32_bf16 v[44:47], v[168:171], v[108:111], v[44:47]\n"
    "ds_read2_b32 v[138:139], v220 offset0:34 offset1:35\n"
    "v_mfma_f32_16x16x32_bf16 v[48:51], v[168:171], v[112:115], v[48:51]\n"
    "ds_read_b128 v[72:75], v31 offset:3712\n"
    "v_mfma_f32_16x16x32_bf16 v[52:55], v[168:171], v[116:119], v[52:55]\n"
    "ds_read_b128 v[76:79], v31 offset:3200\n"
    "v_mfma_f32_16x16x32_bf16 v[56:59], v[168:171], v[120:123], v[56:59]\n"
    "ds_read_b128 v[80:83], v31 offset:2688\n"
    "v_mfma_f32_16x16x32_bf16 v[60:63], v[168:171], v[124:127], v[60:63]\n"
    "ds_read_b128 v[84:87], v31 offset:2176\n"
    "v_mfma_f32_16x16x32_bf16 v[64:67], v[168:171], v[128:131], v[64:67]\n"
    "ds_read_b128 v[88:91], v31 offset:1664\n"
    "v_mfma_f32_16x16x32_bf16 v[68:71], v[168:171], v[132:135], v[68:71]\n"
    "ds_read_b128 v[92:95], v31 offset:1152\n"
    "v_mfma_f32_16x16x32_bf16 v[44:47], v[172:175], v[104:107], v[44:47]\n"
    "ds_read_b128 v[96:99], v31 offset:640\n"
    "v_mfma_f32_16x16x32_bf16 v[48:51], v[172:175], v[108:111], v[48:51]\n"
    "ds_read_b128 v[100:103], v31 offset:128\n"
    "v_mfma_f32_16x16x32_bf16 v[52:55], v[172:175], v[112:115], v[52:55]\n"
    "ds_read2_b32 v[140:141], v220 offset0:160 offset1:161\n"
    "v_mfma_f32_16x16x32_bf16 v[56:59], v[172:175], v[116:119], v[56:59]\n"
    "ds_read2_b32 v[142:143], v220 offset0:162 offset1:163\n"
    "v_mfma_f32_16x16x32_bf16 v[60:63], v[172:175], v[120:123], v[60:63]\n"
    "ds_read2_b32 v[144:145], v221 offset0:32 offset1:33\n"
    "v_mfma_f32_16x16x32_bf16 v[64:67], v[172:175], v[124:127], v[64:67]\n"
    "ds_read2_b32 v[146:147], v221 offset0:34 offset1:35\n"
    "v_mfma_f32_16x16x32_bf16 v[68:71], v[172:175], v[128:131], v[68:71]\n"
    "ds_read2_b32 v[148:149], v221 offset0:160 offset1:161\n"
    "v_mfma_f32_16x16x32_bf16 v[48:51], v[176:179], v[104:107], v[48:51]\n"
    "ds_read2_b32 v[150:151], v221 offset0:162 offset1:163\n"
    "v_mfma_f32_16x16x32_bf16 v[52:55], v[176:179], v[108:111], v[52:55]\n"
    "ds_read2_b32 v[152:153], v222 offset0:32 offset1:33\n"
    "v_mfma_f32_16x16x32_bf16 v[56:59], v[176:179], v[112:115], v[56:59]\n"
    "ds_read2_b32 v[154:155], v222 offset0:34 offset1:35\n"
    "v_mfma_f32_16x16x32_bf16 v[60:63], v[176:179], v[116:119], v[60:63]\n"
    "ds_read2_b32 v[156:157], v222 offset0:160 offset1:161\n"
    "v_mfma_f32_16x16x32_bf16 v[64:67], v[176:179], v[120:123], v[64:67]\n"
    "ds_read2_b32 v[158:159], v222 offset0:162 offset1:163\n"
    "v_mfma_f32_16x16x32_bf16 v[68:71], v[176:179], v[124:127], v[68:71]\n"
    "ds_read2_b32 v[160:161], v223 offset0:32 offset1:33\n"
    "v_mfma_f32_16x16x32_bf16 v[52:55], v[180:183], v[104:107], v[52:55]\n"
    "ds_read2_b32 v[162:163], v223 offset0:34 offset1:35\n"
    "v_mfma_f32_16x16x32_bf16 v[56:59], v[180:183], v[108:111], v[56:59]\n"
    "ds_read2_b32 v[164:165], v223 offset0:160 offset1:161\n"
    "v_mfma_f32_16x16x32_bf16 v[60:63], v[180:183], v[112:115], v[60:63]\n"
    "ds_read2_b32 v[166:167], v223 offset0:162 offset1:163\n"
    "v_mfma_f32_16x16x32_bf16 v[64:67], v[180:183], v[116:119], v[64:67]\n"
    "v_mfma_f32_16x16x32_bf16 v[68:71], v[180:183], v[120:123], v[68:71]\n"
    "v_mfma_f32_16x16x32_bf16 v[56:59], v[184:187], v[104:107], v[56:59]\n"
    "v_mfma_f32_16x16x32_bf16 v[60:63], v[184:187], v[108:111], v[60:63]\n"
    "v_mfma_f32_16x16x32_bf16 v[64:67], v[184:187], v[112:115], v[64:67]\n"
    "v_mfma_f32_16x16x32_bf16 v[68:71], v[184:187], v[116:119], v[68:71]\n"
    "v_mfma_f32_16x16x32_bf16 v[60:63], v[188:191], v[104:107], v[60:63]\n"
    "v_mfma_f32_16x16x32_bf16 v[64:67], v[188:191], v[108:111], v[64:67]\n"
    "v_mfma_f32_16x16x32_bf16 v[68:71], v[188:191], v[112:115], v[68:71]\n"
    "v_mfma_f32_16x16x32_bf16 v[64:67], v[192:195], v[104:107], v[64:67]\n"
    "v_mfma_f32_16x16x32_bf16 v[68:71], v[192:195], v[108:111], v[68:71]\n"
    "v_mfma_f32_16x16x32_bf16 v[68:71], v[196:199], v[104:107], v[68:71]\n"
    "s_waitcnt lgkmcnt(0)\n"
    "v_mfma_f32_16x16x32_bf16 v[40:43], v[136:139], v[72:75], v[40:43]\n"
    "ds_read2_b32 v[168:169], v220 offset0:48 offset1:49\n"
    "v_mfma_f32_16x16x32_bf16 v[44:47], v[136:139], v[76:79], v[44:47]\n"
    "ds_read2_b32 v[170:171], v220 offset0:50 offset1:51\n"
    "v_mfma_f32_16x16x32_bf16 v[48:51], v[136:139], v[80:83], v[48:51]\n"
    "ds_read_b128 v[104:107], v31 offset:3776\n"
    "v_mfma_f32_16x16x32_bf16 v[52:55], v[136:139], v[84:87], v[52:55]\n"
    "ds_read_b128 v[108:111], v31 offset:3264\n"
    "v_mfma_f32_16x16x32_bf16 v[56:59], v[136:139], v[88:91], v[56:59]\n"
    "ds_read_b128 v[112:115], v31 offset:2752\n"
    "v_mfma_f32_16x16x32_bf16 v[60:63], v[136:139], v[92:95], v[60:63]\n"
    "ds_read_b128 v[116:119], v31 offset:2240\n"
    "v_mfma_f32_16x16x32_bf16 v[64:67], v[136:139], v[96:99], v[64:67]\n"
    "ds_read_b128 v[120:123], v31 offset:1728\n"
    "v_mfma_f32_16x16x32_bf16 v[68:71], v[136:139], v[100:103], v[68:71]\n"
    "ds_read_b128 v[124:127], v31 offset:1216\n"
    "v_mfma_f32_16x16x32_bf16 v[44:47], v[140:143], v[72:75], v[44:47]\n"
    "ds_read_b128 v[128:131], v31 offset:704\n"
    "v_mfma_f32_16x16x32_bf16 v[48:51], v[140:143], v[76:79], v[48:51]\n"
    "ds_read_b128 v[132:135], v31 offset:192\n"
    "v_mfma_f32_16x16x32_bf16 v[52:55], v[140:143], v[80:83], v[52:55]\n"
    "ds_read2_b32 v[172:173], v220 offset0:176 offset1:177\n"
    "v_mfma_f32_16x16x32_bf16 v[56:59], v[140:143], v[84:87], v[56:59]\n"
    "ds_read2_b32 v[174:175], v220 offset0:178 offset1:179\n"
    "v_mfma_f32_16x16x32_bf16 v[60:63], v[140:143], v[88:91], v[60:63]\n"
    "ds_read2_b32 v[176:177], v221 offset0:48 offset1:49\n"
    "v_mfma_f32_16x16x32_bf16 v[64:67], v[140:143], v[92:95], v[64:67]\n"
    "ds_read2_b32 v[178:179], v221 offset0:50 offset1:51\n"
    "v_mfma_f32_16x16x32_bf16 v[68:71], v[140:143], v[96:99], v[68:71]\n"
    "ds_read2_b32 v[180:181], v221 offset0:176 offset1:177\n"
    "v_mfma_f32_16x16x32_bf16 v[48:51], v[144:147], v[72:75], v[48:51]\n"
    "ds_read2_b32 v[182:183], v221 offset0:178 offset1:179\n"
    "v_mfma_f32_16x16x32_bf16 v[52:55], v[144:147], v[76:79], v[52:55]\n"
    "ds_read2_b32 v[184:185], v222 offset0:48 offset1:49\n"
    "v_mfma_f32_16x16x32_bf16 v[56:59], v[144:147], v[80:83], v[56:59]\n"
    "ds_read2_b32 v[186:187], v222 offset0:50 offset1:51\n"
    "v_mfma_f32_16x16x32_bf16 v[60:63], v[144:147], v[84:87], v[60:63]\n"
    "ds_read2_b32 v[188:189], v222 offset0:176 offset1:177\n"
    "v_mfma_f32_16x16x32_bf16 v[64:67], v[144:147], v[88:91], v[64:67]\n"
    "ds_read2_b32 v[190:191], v222 offset0:178 offset1:179\n"
    "v_mfma_f32_16x16x32_bf16 v[68:71], v[144:147], v[92:95], v[68:71]\n"
    "ds_read2_b32 v[192:193], v223 offset0:48 offset1:49\n"
    "v_mfma_f32_16x16x32_bf16 v[52:55], v[148:151], v[72:75], v[52:55]\n"
    "ds_read2_b32 v[194:195], v223 offset0:50 offset1:51\n"
    "v_mfma_f32_16x16x32_bf16 v[56:59], v[148:151], v[76:79], v[56:59]\n"
    "ds_read2_b32 v[196:197], v223 offset0:176 offset1:177\n"
    "v_mfma_f32_16x16x32_bf16 v[60:63], v[148:151], v[80:83], v[60:63]\n"
    "ds_read2_b32 v[198:199], v223 offset0:178 offset1:179\n"
    "v_mfma_f32_16x16x32_bf16 v[64:67], v[148:151], v[84:87], v[64:67]\n"
    "v_mfma_f32_16x16x32_bf16 v[68:71], v[148:151], v[88:91], v[68:71]\n"
    "v_mfma_f32_16x16x32_bf16 v[56:59], v[152:155], v[72:75], v[56:59]\n"
    "v_mfma_f32_16x16x32_bf16 v[60:63], v[152:155], v[76:79], v[60:63]\n"
    "v_mfma_f32_16x16x32_bf16 v[64:67], v[152:155], v[80:83], v[64:67]\n"
    "v_mfma_f32_16x16x32_bf16 v[68:71], v[152:155], v[84:87], v[68:71]\n"
    "v_mfma_f32_16x16x32_bf16 v[60:63], v[156:159], v[72:75], v[60:63]\n"
    "v_mfma_f32_16x16x32_bf16 v[64:67], v[156:159], v[76:79], v[64:67]\n"
    "v_mfma_f32_16x16x32_bf16 v[68:71], v[156:159], v[80:83], v[68:71]\n"
    "v_mfma_f32_16x16x32_bf16 v[64:67], v[160:163], v[72:75], v[64:67]\n"
    "v_mfma_f32_16x16x32_bf16 v[68:71], v[160:163], v[76:79], v[68:71]\n"
    "v_mfma_f32_16x16x32_bf16 v[68:71], v[164:167], v[72:75], v[68:71]\n"
    "s_waitcnt lgkmcnt(0)\n"
    "ds_inc_u32 v2, v2\n"
    "v_mfma_f32_16x16x32_bf16 v[40:43], v[168:171], v[104:107], v[40:43]\n"
    "v_mfma_f32_16x16x32_bf16 v[44:47], v[168:171], v[108:111], v[44:47]\n"
    "v_mfma_f32_16x16x32_bf16 v[48:51], v[168:171], v[112:115], v[48:51]\n"
    "v_mfma_f32_16x16x32_bf16 v[52:55], v[168:171], v[116:119], v[52:55]\n"
    "v_mfma_f32_16x16x32_bf16 v[56:59], v[168:171], v[120:123], v[56:59]\n"
    "v_mfma_f32_16x16x32_bf16 v[60:63], v[168:171], v[124:127], v[60:63]\n"
    "v_mfma_f32_16x16x32_bf16 v[64:67], v[168:171], v[128:131], v[64:67]\n"
    "v_mfma_f32_16x16x32_bf16 v[68:71], v[168:171], v[132:135], v[68:71]\n"
    "v_mfma_f32_16x16x32_bf16 v[44:47], v[172:175], v[104:107], v[44:47]\n"
    "v_mfma_f32_16x16x32_bf16 v[48:51], v[172:175], v[108:111], v[48:51]\n"
    "v_mfma_f32_16x16x32_bf16 v[52:55], v[172:175], v[112:115], v[52:55]\n"
    "v_mfma_f32_16x16x32_bf16 v[56:59], v[172:175], v[116:119], v[56:59]\n"
    "v_mfma_f32_16x16x32_bf16 v[60:63], v[172:175], v[120:123], v[60:63]\n"
    "v_mfma_f32_16x16x32_bf16 v[64:67], v[172:175], v[124:127], v[64:67]\n"
    "v_mfma_f32_16x16x32_bf16 v[68:71], v[172:175], v[128:131], v[68:71]\n"
    "v_mfma_f32_16x16x32_bf16 v[48:51], v[176:179], v[104:107], v[48:51]\n"
    "v_mfma_f32_16x16x32_bf16 v[52:55], v[176:179], v[108:111], v[52:55]\n"
    "v_mfma_f32_16x16x32_bf16 v[56:59], v[176:179], v[112:115], v[56:59]\n"
    "v_mfma_f32_16x16x32_bf16 v[60:63], v[176:179], v[116:119], v[60:63]\n"
    "v_mfma_f32_16x16x32_bf16 v[64:67], v[176:179], v[120:123], v[64:67]\n"
    "v_mfma_f32_16x16x32_bf16 v[68:71], v[176:179], v[124:127], v[68:71]\n"
    "v_mfma_f32_16x16x32_bf16 v[52:55], v[180:183], v[104:107], v[52:55]\n"
    "v_mfma_f32_16x16x32_bf16 v[56:59], v[180:183], v[108:111], v[56:59]\n"
    "v_mfma_f32_16x16x32_bf16 v[60:63], v[180:183], v[112:115], v[60:63]\n"
    "v_mfma_f32_16x16x32_bf16 v[64:67], v[180:183], v[116:119], v[64:67]\n"
    "v_mfma_f32_16x16x32_bf16 v[68:71], v[180:183], v[120:123], v[68:71]\n"
    "ds_read_b32 v3, v9 offset:0\n"
    "v_mfma_f32_16x16x32_bf16 v[56:59], v[184:187], v[104:107], v[56:59]\n"
    "v_mfma_f32_16x16x32_bf16 v[60:63], v[184:187], v[108:111], v[60:63]\n"
    "v_mfma_f32_16x16x32_bf16 v[64:67], v[184:187], v[112:115], v[64:67]\n"
    "v_mfma_f32_16x16x32_bf16 v[68:71], v[184:187], v[116:119], v[68:71]\n"
    "v_mfma_f32_16x16x32_bf16 v[60:63], v[188:191], v[104:107], v[60:63]\n"
    "v_mfma_f32_16x16x32_bf16 v[64:67], v[188:191], v[108:111], v[64:67]\n"
    "v_mfma_f32_16x16x32_bf16 v[68:71], v[188:191], v[112:115], v[68:71]\n"
    "v_mfma_f32_16x16x32_bf16 v[64:67], v[192:195], v[104:107], v[64:67]\n"
    "v_mfma_f32_16x16x32_bf16 v[68:71], v[192:195], v[108:111], v[68:71]\n"
    "v_mfma_f32_16x16x32_bf16 v[68:71], v[196:199], v[104:107], v[68:71]\n"
    "s_waitcnt lgkmcnt(0)\n"
    "v_cmp_gt_u32 vcc, 5, v3\n"
    "s_cbranch_vccnz .Ls4_cs89\n"
    ".Ls4_cb90:\n"
    "s_cmp_eq_u32 s81, 0\n"
    "s_cbranch_scc0 .Ls4_y191\n"
    "ds_write_b128 v215, v[56:59] offset:0\n"
    "ds_write_b128 v215, v[60:63] offset:1024\n"
    "ds_write_b128 v215, v[64:67] offset:2048\n"
    "ds_write_b128 v215, v[68:71] offset:3072\n"
    "ds_inc_u32 v2, v2\n"
    "ds_read_b32 v3, v9 offset:0\n"
    "s_waitcnt lgkmcnt(0)\n"
    "v_cmp_gt_u32 vcc, 6, v3\n"
    "s_cbranch_vccnz .Ls4_cs93\n"
    ".Ls4_cb94:\n"
    "ds_read_b128 v[80:83], v216 offset:0\n"
    "ds_read_b128 v[84:87], v216 offset:1024\n"
    "ds_read_b128 v[88:91], v216 offset:2048\n"
    "ds_read_b128 v[92:95], v216 offset:3072\n"
    "ds_inc_u32 v2, v2\n"
    "s_waitcnt lgkmcnt(4)\n"
    "v_add_f32 v80, v80, v40\n"
    "v_add_f32 v81, v81, v41\n"
    "v_add_f32 v82, v82, v42\n"
    "v_add_f32 v83, v83, v43\n"
    "v_cvt_pk_bf16_f32 v72, v80, v81\n"
    "v_cvt_pk_bf16_f32 v73, v82, v83\n"
    "s_waitcnt lgkmcnt(3)\n"
    "v_add_f32 v84, v84, v44\n"
    "v_add_f32 v85, v85, v45\n"
    "v_add_f32 v86, v86, v46\n"
    "v_add_f32 v87, v87, v47\n"
    "v_cvt_pk_bf16_f32 v74, v84, v85\n"
    "v_cvt_pk_bf16_f32 v75, v86, v87\n"
    "s_waitcnt lgkmcnt(2)\n"
    "v_add_f32 v88, v88, v48\n"
    "v_add_f32 v89, v89, v49\n"
    "v_add_f32 v90, v90, v50\n"
    "v_add_f32 v91, v91, v51\n"
    "v_cvt_pk_bf16_f32 v76, v88, v89\n"
    "v_cvt_pk_bf16_f32 v77, v90, v91\n"
    "s_waitcnt lgkmcnt(1)\n"
    "v_add_f32 v92, v92, v52\n"
    "v_add_f32 v93, v93, v53\n"
    "v_add_f32 v94, v94, v54\n"
    "v_add_f32 v95, v95, v55\n"
    "v_cvt_pk_bf16_f32 v78, v92, v93\n"
    "v_cvt_pk_bf16_f32 v79, v94, v95\n"
    "s_branch .Ls4_yj92\n"
    ".Ls4_y191:\n"
    "ds_write_b128 v215, v[40:43] offset:0\n"
    "ds_write_b128 v215, v[44:47] offset:1024\n"
    "ds_write_b128 v215, v[48:51] offset:2048\n"
    "ds_write_b128 v215, v[52:55] offset:3072\n"
    "ds_inc_u32 v2, v2\n"
    "ds_read_b32 v3, v9 offset:0\n"
    "s_waitcnt lgkmcnt(0)\n"
    "v_cmp_gt_u32 vcc, 6, v3\n"
    "s_cbranch_vccnz .Ls4_cs95\n"
    ".Ls4_cb96:\n"
    "ds_read_b128 v[80:83], v216 offset:0\n"
    "ds_read_b128 v[84:87], v216 offset:1024\n"
    "ds_read_b128 v[88:91], v216 offset:2048\n"
    "ds_read_b128 v[92:95], v216 offset:3072\n"
    "ds_inc_u32 v2, v2\n"
    "s_waitcnt lgkmcnt(4)\n"
    "v_add_f32 v80, v80, v56\n"
    "v_add_f32 v81, v81, v57\n"
    "v_add_f32 v82, v82, v58\n"
    "v_add_f32 v83, v83, v59\n"
    "v_cvt_pk_bf16_f32 v72, v80, v81\n"
    "v_cvt_pk_bf16_f32 v73, v82, v83\n"
    "s_waitcnt lgkmcnt(3)\n"
    "v_add_f32 v84, v84, v60\n"
    "v_add_f32 v85, v85, v61\n"
    "v_add_f32 v86, v86, v62\n"
    "v_add_f32 v87, v87, v63\n"
    "v_cvt_pk_bf16_f32 v74, v84, v85\n"
    "v_cvt_pk_bf16_f32 v75, v86, v87\n"
    "s_waitcnt lgkmcnt(2)\n"
    "v_add_f32 v88, v88, v64\n"
    "v_add_f32 v89, v89, v65\n"
    "v_add_f32 v90, v90, v66\n"
    "v_add_f32 v91, v91, v67\n"
    "v_cvt_pk_bf16_f32 v76, v88, v89\n"
    "v_cvt_pk_bf16_f32 v77, v90, v91\n"
    "s_waitcnt lgkmcnt(1)\n"
    "v_add_f32 v92, v92, v68\n"
    "v_add_f32 v93, v93, v69\n"
    "v_add_f32 v94, v94, v70\n"
    "v_add_f32 v95, v95, v71\n"
    "v_cvt_pk_bf16_f32 v78, v92, v93\n"
    "v_cvt_pk_bf16_f32 v79, v94, v95\n"
    ".Ls4_yj92:\n"
    "v_permlane16_swap_b32 v72, v74\n"
    "v_permlane16_swap_b32 v73, v75\n"
    "buffer_store_dwordx4 v[72:75], v217, s[24:27], s62 offen offset:0 sc1\n"
    "v_permlane16_swap_b32 v76, v78\n"
    "v_permlane16_swap_b32 v77, v79\n"
    "buffer_store_dwordx4 v[76:79], v217, s[24:27], s62 offen offset:1024 sc1\n"
    "s_lshl_b32 s83, s15, 12\n"
    "s_add_u32 s83, s83, 81920\n"
    "v_bfe_u32 v21, v12, 2, 2\n"
    "v_lshl_add_u32 v22, v14, 2, v21\n"
    "v_lshlrev_b32 v22, 7, v22\n"
    "v_and_b32 v23, 3, v12\n"
    "v_lshl_add_u32 v22, v23, 3, v22\n"
    "v_lshrrev_b32 v21, 1, v21\n"
    "v_and_b32 v23, 1, v14\n"
    "v_lshl_or_b32 v21, v23, 1, v21\n"
    "v_lshl_add_u32 v22, v21, 5, v22\n"
    "v_add_u32 v16, s83, v22\n"
    "v_xor_b32 v17, 32, v16\n"
    "v_xor_b32 v18, 64, v16\n"
    "v_xor_b32 v19, 96, v16\n"
    "s_mul_i32 s84, s15, 11\n"
    "s_mul_i32 s89, s101, 1\n"
    "s_sub_u32 s90, s89, 11\n"
    "s_cselect_b32 s89, s89, s90\n"
    "s_add_u32 s48, s84, s89\n"
    "s_add_u32 s89, s89, 1\n"
    "s_cmp_eq_u32 s89, 11\n"
    "s_cselect_b32 s89, 0, s89\n"
    "s_add_u32 s49, s84, s89\n"
    "s_add_u32 s89, s89, 1\n"
    "s_cmp_eq_u32 s89, 11\n"
    "s_cselect_b32 s89, 0, s89\n"
    "s_add_u32 s50, s84, s89\n"
    "s_add_u32 s89, s89, 1\n"
    "s_cmp_eq_u32 s89, 11\n"
    "s_cselect_b32 s89, 0, s89\n"
    "s_add_u32 s51, s84, s89\n"
    "s_add_u32 s89, s89, 1\n"
    "s_cmp_eq_u32 s89, 11\n"
    "s_cselect_b32 s89, 0, s89\n"
    "s_add_u32 s52, s84, s89\n"
    "s_add_u32 s89, s89, 1\n"
    "s_cmp_eq_u32 s89, 11\n"
    "s_cselect_b32 s89, 0, s89\n"
    "s_add_u32 s53, s84, s89\n"
    "s_add_u32 s89, s89, 1\n"
    "s_cmp_eq_u32 s89, 11\n"
    "s_cselect_b32 s89, 0, s89\n"
    "s_add_u32 s54, s84, s89\n"
    "s_add_u32 s89, s89, 1\n"
    "s_cmp_eq_u32 s89, 11\n"
    "s_cselect_b32 s89, 0, s89\n"
    "s_add_u32 s55, s84, s89\n"
    "s_add_u32 s89, s89, 1\n"
    "s_cmp_eq_u32 s89, 11\n"
    "s_cselect_b32 s89, 0, s89\n"
    "s_add_u32 s56, s84, s89\n"
    "s_add_u32 s89, s89, 1\n"
    "s_cmp_eq_u32 s89, 11\n"
    "s_cselect_b32 s89, 0, s89\n"
    "s_add_u32 s57, s84, s89\n"
    "s_add_u32 s89, s89, 1\n"
    "s_cmp_eq_u32 s89, 11\n"
    "s_cselect_b32 s89, 0, s89\n"
    "s_add_u32 s58, s84, s89\n"
    "s_mov_b32 s66, 0x92492492\n"
    "s_mov_b32 s67, 0x24924924\n"
    "v_lshrrev_b32 v23, 3, v12\n"
    "v_and_b32 v24, 7, v12\n"
    "v_lshlrev_b32 v22, 4, v24\n"
    "v_lshl_add_u32 v22, v23, 10, v22\n"
    "v_lshrrev_b32 v25, 5, v12\n"
    "s_lshl_b32 s89, s15, 2\n"
    "v_add_u32 v25, s89, v25\n"
    "v_and_b32 v25, 7, v25\n"
    "v_xor_b32 v24, v24, v25\n"
    "v_lshlrev_b32 v24, 4, v24\n"
    "v_lshl_add_u32 v20, v23, 12, v24\n"
    "v_xor_b32 v21, 32, v20\n"
    "v_add_u32 v21, 31744, v21\n"
    "v_add_u32 v23, 7168, v22\n"
    "v_lshlrev_b32 v24, 2, v12\n"
    "v_and_b32 v25, 7, v12\n"
    "v_lshlrev_b32 v25, 7, v25\n"
    "v_add_u32 v11, 160064, v24\n"
    "v_mov_b32 v9, 160064\n"
    "ds_read_b32 v3, v1 offset:1024\n"
    "s_waitcnt lgkmcnt(0)\n"
    "v_cmp_gt_u32 vcc, 7, v3\n"
    "s_cbranch_vccnz .Ls4_cs97\n"
    ".Ls4_cb98:\n"
    "s_lshl4_add_u32 s89, s15, 0\n"
    "s_lshl_b32 s91, s15, 11\n"
    "s_lshl_b32 s92, s13, 6\n"
    "s_add_u32 m0, s91, 149568\n"
    "s_add_u32 s92, s92, s89\n"
    "s_lshl_b32 s92, s92, 10\n"
    "s_add_u32 s92, s92, s77\n"
    "buffer_load_dwordx4 v22, s[16:19], s92 offen lds\n"
    "buffer_load_dwordx4 v23, s[16:19], s92 offen offset:1024 lds\n"
    "s_mov_b32 m0, 159808\n"
    "s_nop 0\n"
    "buffer_load_dword v24, s[44:47], s77 offen lds\n"
    "s_mov_b64 exec, 0xff\n"
    "s_waitcnt vmcnt(3)\n"
    "ds_inc_rtn_u32 v6, v11, v2 offset:192\n"
    "s_mov_b64 exec, -1\n"
    "ds_read_b32 v3, v1 offset:0\n"
    "s_mov_b64 exec, 0xff\n"
    "s_waitcnt lgkmcnt(1)\n"
    "v_cmpx_eq_u32 vcc, 3, v6\n"
    "buffer_store_dwordx4 v[4:7], v0, s[24:27], s63 offen sc1\n"
    "s_mov_b64 exec, -1\n"
    "s_waitcnt lgkmcnt(0)\n"
    "v_cmp_gt_u32 vcc, 16, v3\n"
    "s_cbranch_vccnz .Ls4_cs99\n"
    ".Ls4_cb100:\n"
    "s_lshl_b32 s89, s48, 15\n"
    "s_add_u32 m0, s83, 0\n"
    "s_add_u32 s89, s89, s77\n"
    "s_bitcmp1_b64 s[66:67], s48\n"
    "s_cselect_b32 s94, 8192, 0\n"
    "s_add_u32 s93, s89, s94\n"
    "s_sub_u32 s94, s89, s94\n"
    "buffer_load_dwordx4 v36, s[36:39], s89 offen offset:0 lds\n"
    "buffer_load_dwordx4 v37, s[36:39], s93 offen offset:1024 lds\n"
    "buffer_load_dwordx4 v38, s[36:39], s94 offen offset:2048 lds\n"
    "buffer_load_dwordx4 v39, s[36:39], s89 offen offset:3072 lds\n"
    "s_lshl_b32 s89, s49, 15\n"
    "s_add_u32 m0, s83, 16384\n"
    "s_add_u32 s89, s89, s77\n"
    "s_bitcmp1_b64 s[66:67], s49\n"
    "s_cselect_b32 s94, 8192, 0\n"
    "s_add_u32 s93, s89, s94\n"
    "s_sub_u32 s94, s89, s94\n"
    "buffer_load_dwordx4 v36, s[36:39], s89 offen offset:0 lds\n"
    "buffer_load_dwordx4 v37, s[36:39], s93 offen offset:1024 lds\n"
    "buffer_load_dwordx4 v38, s[36:39], s94 offen offset:2048 lds\n"
    "buffer_load_dwordx4 v39, s[36:39], s89 offen offset:3072 lds\n"
    "s_waitcnt vmcnt(4)\n"
    "ds_read_b64_tr_b16 v[80:81], v16 offset:0\n"
    "ds_read_b64_tr_b16 v[82:83], v16 offset:2048\n"
    "ds_read_b64_tr_b16 v[84:85], v17 offset:0\n"
    "ds_read_b64_tr_b16 v[86:87], v17 offset:2048\n"
    "ds_read_b64_tr_b16 v[88:89], v18 offset:0\n"
    "ds_read_b64_tr_b16 v[90:91], v18 offset:2048\n"
    "ds_read_b64_tr_b16 v[92:93], v19 offset:0\n"
    "ds_read_b64_tr_b16 v[94:95], v19 offset:2048\n"
    "s_waitcnt lgkmcnt(0)\n"
    "s_lshl_b32 s89, s50, 15\n"
    "s_add_u32 m0, s83, 0\n"
    "s_add_u32 s89, s89, s77\n"
    "s_bitcmp1_b64 s[66:67], s50\n"
    "s_cselect_b32 s94, 8192, 0\n"
    "s_add_u32 s93, s89, s94\n"
    "s_sub_u32 s94, s89, s94\n"
    "buffer_load_dwordx4 v36, s[36:39], s89 offen offset:0 lds\n"
    "buffer_load_dwordx4 v37, s[36:39], s93 offen offset:1024 lds\n"
    "buffer_load_dwordx4 v38, s[36:39], s94 offen offset:2048 lds\n"
    "buffer_load_dwordx4 v39, s[36:39], s89 offen offset:3072 lds\n"
    "s_waitcnt vmcnt(4)\n"
    "ds_read_b64_tr_b16 v[96:97], v16 offset:16384\n"
    "ds_read_b64_tr_b16 v[98:99], v16 offset:18432\n"
    "ds_read_b64_tr_b16 v[100:101], v17 offset:16384\n"
    "ds_read_b64_tr_b16 v[102:103], v17 offset:18432\n"
    "ds_read_b64_tr_b16 v[104:105], v18 offset:16384\n"
    "ds_read_b64_tr_b16 v[106:107], v18 offset:18432\n"
    "ds_read_b64_tr_b16 v[108:109], v19 offset:16384\n"
    "ds_read_b64_tr_b16 v[110:111], v19 offset:18432\n"
    "s_waitcnt lgkmcnt(0)\n"
    "s_lshl_b32 s89, s51, 15\n"
    "s_add_u32 m0, s83, 16384\n"
    "s_add_u32 s89, s89, s77\n"
    "s_bitcmp1_b64 s[66:67], s51\n"
    "s_cselect_b32 s94, 8192, 0\n"
    "s_add_u32 s93, s89, s94\n"
    "s_sub_u32 s94, s89, s94\n"
    "buffer_load_dwordx4 v36, s[36:39], s89 offen offset:0 lds\n"
    "buffer_load_dwordx4 v37, s[36:39], s93 offen offset:1024 lds\n"
    "buffer_load_dwordx4 v38, s[36:39], s94 offen offset:2048 lds\n"
    "buffer_load_dwordx4 v39, s[36:39], s89 offen offset:3072 lds\n"
    "s_waitcnt vmcnt(4)\n"
    "ds_read_b64_tr_b16 v[112:113], v16 offset:0\n"
    "ds_read_b64_tr_b16 v[114:115], v16 offset:2048\n"
    "ds_read_b64_tr_b16 v[116:117], v17 offset:0\n"
    "ds_read_b64_tr_b16 v[118:119], v17 offset:2048\n"
    "ds_read_b64_tr_b16 v[120:121], v18 offset:0\n"
    "ds_read_b64_tr_b16 v[122:123], v18 offset:2048\n"
    "ds_read_b64_tr_b16 v[124:125], v19 offset:0\n"
    "ds_read_b64_tr_b16 v[126:127], v19 offset:2048\n"
    "s_waitcnt lgkmcnt(0)\n"
    "s_lshl_b32 s89, s52, 15\n"
    "s_add_u32 m0, s83, 0\n"
    "s_add_u32 s89, s89, s77\n"
    "s_bitcmp1_b64 s[66:67], s52\n"
    "s_cselect_b32 s94, 8192, 0\n"
    "s_add_u32 s93, s89, s94\n"
    "s_sub_u32 s94, s89, s94\n"
    "buffer_load_dwordx4 v36, s[36:39], s89 offen offset:0 lds\n"
    "buffer_load_dwordx4 v37, s[36:39], s93 offen offset:1024 lds\n"
    "buffer_load_dwordx4 v38, s[36:39], s94 offen offset:2048 lds\n"
    "buffer_load_dwordx4 v39, s[36:39], s89 offen offset:3072 lds\n"
    "s_waitcnt vmcnt(4)\n"
    "ds_read_b64_tr_b16 v[128:129], v16 offset:16384\n"
    "ds_read_b64_tr_b16 v[130:131], v16 offset:18432\n"
    "ds_read_b64_tr_b16 v[132:133], v17 offset:16384\n"
    "ds_read_b64_tr_b16 v[134:135], v17 offset:18432\n"
    "ds_read_b64_tr_b16 v[136:137], v18 offset:16384\n"
    "ds_read_b64_tr_b16 v[138:139], v18 offset:18432\n"
    "ds_read_b64_tr_b16 v[140:141], v19 offset:16384\n"
    "ds_read_b64_tr_b16 v[142:143], v19 offset:18432\n"
    "s_waitcnt lgkmcnt(0)\n"
    "s_lshl_b32 s89, s53, 15\n"
    "s_add_u32 m0, s83, 16384\n"
    "s_add_u32 s89, s89, s77\n"
    "s_bitcmp1_b64 s[66:67], s53\n"
    "s_cselect_b32 s94, 8192, 0\n"
    "s_add_u32 s93, s89, s94\n"
    "s_sub_u32 s94, s89, s94\n"
    "buffer_load_dwordx4 v36, s[36:39], s89 offen offset:0 lds\n"
    "buffer_load_dwordx4 v37, s[36:39], s93 offen offset:1024 lds\n"
    "buffer_load_dwordx4 v38, s[36:39], s94 offen offset:2048 lds\n"
    "buffer_load_dwordx4 v39, s[36:39], s89 offen offset:3072 lds\n"
    "s_waitcnt vmcnt(4)\n"
    "ds_read_b64_tr_b16 v[144:145], v16 offset:0\n"
    "ds_read_b64_tr_b16 v[146:147], v16 offset:2048\n"
    "ds_read_b64_tr_b16 v[148:149], v17 offset:0\n"
    "ds_read_b64_tr_b16 v[150:151], v17 offset:2048\n"
    "ds_read_b64_tr_b16 v[152:153], v18 offset:0\n"
    "ds_read_b64_tr_b16 v[154:155], v18 offset:2048\n"
    "ds_read_b64_tr_b16 v[156:157], v19 offset:0\n"
    "ds_read_b64_tr_b16 v[158:159], v19 offset:2048\n"
    "s_waitcnt lgkmcnt(0)\n"
    "s_lshl_b32 s89, s54, 15\n"
    "s_add_u32 m0, s83, 0\n"
    "s_add_u32 s89, s89, s77\n"
    "s_bitcmp1_b64 s[66:67], s54\n"
    "s_cselect_b32 s94, 8192, 0\n"
    "s_add_u32 s93, s89, s94\n"
    "s_sub_u32 s94, s89, s94\n"
    "buffer_load_dwordx4 v36, s[36:39], s89 offen offset:0 lds\n"
    "buffer_load_dwordx4 v37, s[36:39], s93 offen offset:1024 lds\n"
    "buffer_load_dwordx4 v38, s[36:39], s94 offen offset:2048 lds\n"
    "buffer_load_dwordx4 v39, s[36:39], s89 offen offset:3072 lds\n"
    "s_waitcnt vmcnt(4)\n"
    "ds_read_b64_tr_b16 v[160:161], v16 offset:16384\n"
    "ds_read_b64_tr_b16 v[162:163], v16 offset:18432\n"
    "ds_read_b64_tr_b16 v[164:165], v17 offset:16384\n"
    "ds_read_b64_tr_b16 v[166:167], v17 offset:18432\n"
    "ds_read_b64_tr_b16 v[168:169], v18 offset:16384\n"
    "ds_read_b64_tr_b16 v[170:171], v18 offset:18432\n"
    "ds_read_b64_tr_b16 v[172:173], v19 offset:16384\n"
    "ds_read_b64_tr_b16 v[174:175], v19 offset:18432\n"
    "s_waitcnt lgkmcnt(0)\n"
    "s_lshl_b32 s89, s55, 15\n"
    "s_add_u32 m0, s83, 16384\n"
    "s_add_u32 s89, s89, s77\n"
    "s_bitcmp1_b64 s[66:67], s55\n"
    "s_cselect_b32 s94, 8192, 0\n"
    "s_add_u32 s93, s89, s94\n"
    "s_sub_u32 s94, s89, s94\n"
    "buffer_load_dwordx4 v36, s[36:39], s89 offen offset:0 lds\n"
    "buffer_load_dwordx4 v37, s[36:39], s93 offen offset:1024 lds\n"
    "buffer_load_dwordx4 v38, s[36:39], s94 offen offset:2048 lds\n"
    "buffer_load_dwordx4 v39, s[36:39], s89 offen offset:3072 lds\n"
    "s_waitcnt vmcnt(4)\n"
    "ds_read_b64_tr_b16 v[176:177], v16 offset:0\n"
    "ds_read_b64_tr_b16 v[178:179], v16 offset:2048\n"
    "ds_read_b64_tr_b16 v[180:181], v17 offset:0\n"
    "ds_read_b64_tr_b16 v[182:183], v17 offset:2048\n"
    "ds_read_b64_tr_b16 v[184:185], v18 offset:0\n"
    "ds_read_b64_tr_b16 v[186:187], v18 offset:2048\n"
    "ds_read_b64_tr_b16 v[188:189], v19 offset:0\n"
    "ds_read_b64_tr_b16 v[190:191], v19 offset:2048\n"
    "s_waitcnt lgkmcnt(0)\n"
    "s_lshl_b32 s89, s56, 15\n"
    "s_add_u32 m0, s83, 0\n"
    "s_add_u32 s89, s89, s77\n"
    "s_bitcmp1_b64 s[66:67], s56\n"
    "s_cselect_b32 s94, 8192, 0\n"
    "s_add_u32 s93, s89, s94\n"
    "s_sub_u32 s94, s89, s94\n"
    "buffer_load_dwordx4 v36, s[36:39], s89 offen offset:0 lds\n"
    "buffer_load_dwordx4 v37, s[36:39], s93 offen offset:1024 lds\n"
    "buffer_load_dwordx4 v38, s[36:39], s94 offen offset:2048 lds\n"
    "buffer_load_dwordx4 v39, s[36:39], s89 offen offset:3072 lds\n"
    "s_waitcnt vmcnt(4)\n"
    "ds_read_b64_tr_b16 v[192:193], v16 offset:16384\n"
    "ds_read_b64_tr_b16 v[194:195], v16 offset:18432\n"
    "ds_read_b64_tr_b16 v[196:197], v17 offset:16384\n"
    "ds_read_b64_tr_b16 v[198:199], v17 offset:18432\n"
    "ds_read_b64_tr_b16 v[200:201], v18 offset:16384\n"
    "ds_read_b64_tr_b16 v[202:203], v18 offset:18432\n"
    "ds_read_b64_tr_b16 v[204:205], v19 offset:16384\n"
    "ds_read_b64_tr_b16 v[206:207], v19 offset:18432\n"
    "s_waitcnt lgkmcnt(0)\n"
    "s_lshl_b32 s89, s57, 15\n"
    "s_add_u32 m0, s83, 16384\n"
    "s_add_u32 s89, s89, s77\n"
    "s_bitcmp1_b64 s[66:67], s57\n"
    "s_cselect_b32 s94, 8192, 0\n"
    "s_add_u32 s93, s89, s94\n"
    "s_sub_u32 s94, s89, s94\n"
    "buffer_load_dwordx4 v36, s[36:39], s89 offen offset:0 lds\n"
    "buffer_load_dwordx4 v37, s[36:39], s93 offen offset:1024 lds\n"
    "buffer_load_dwordx4 v38, s[36:39], s94 offen offset:2048 lds\n"
    "buffer_load_dwordx4 v39, s[36:39], s89 offen offset:3072 lds\n"
    "buffer_load_dwordx2 v[6:7], v25, s[24:27], s64 offen sc1\n"
    "s_waitcnt vmcnt(5)\n"
    "ds_read_b64_tr_b16 v[208:209], v16 offset:0\n"
    "ds_read_b64_tr_b16 v[210:211], v16 offset:2048\n"
    "ds_read_b64_tr_b16 v[212:213], v17 offset:0\n"
    "ds_read_b64_tr_b16 v[214:215], v17 offset:2048\n"
    "ds_read_b64_tr_b16 v[216:217], v18 offset:0\n"
    "ds_read_b64_tr_b16 v[218:219], v18 offset:2048\n"
    "ds_read_b64_tr_b16 v[220:221], v19 offset:0\n"
    "ds_read_b64_tr_b16 v[222:223], v19 offset:2048\n"
    "s_waitcnt lgkmcnt(0)\n"
    "s_lshl_b32 s89, s58, 15\n"
    "s_add_u32 m0, s83, 0\n"
    "s_add_u32 s89, s89, s77\n"
    "s_bitcmp1_b64 s[66:67], s58\n"
    "s_cselect_b32 s94, 8192, 0\n"
    "s_add_u32 s93, s89, s94\n"
    "s_sub_u32 s94, s89, s94\n"
    "buffer_load_dwordx4 v36, s[36:39], s89 offen offset:0 lds\n"
    "buffer_load_dwordx4 v37, s[36:39], s93 offen offset:1024 lds\n"
    "buffer_load_dwordx4 v38, s[36:39], s94 offen offset:2048 lds\n"
    "buffer_load_dwordx4 v39, s[36:39], s89 offen offset:3072 lds\n"
    "s_waitcnt vmcnt(5)\n"
    "ds_read_b64_tr_b16 v[224:225], v16 offset:16384\n"
    "ds_read_b64_tr_b16 v[226:227], v16 offset:18432\n"
    "ds_read_b64_tr_b16 v[228:229], v17 offset:16384\n"
    "ds_read_b64_tr_b16 v[230:231], v17 offset:18432\n"
    "ds_read_b64_tr_b16 v[232:233], v18 offset:16384\n"
    "ds_read_b64_tr_b16 v[234:235], v18 offset:18432\n"
    "ds_read_b64_tr_b16 v[236:237], v19 offset:16384\n"
    "ds_read_b64_tr_b16 v[238:239], v19 offset:18432\n"
    "s_waitcnt lgkmcnt(0)\n"
    "s_waitcnt vmcnt(0)\n"
    "ds_read_b64_tr_b16 v[240:241], v16 offset:0\n"
    "ds_read_b64_tr_b16 v[242:243], v16 offset:2048\n"
    "ds_read_b64_tr_b16 v[244:245], v17 offset:0\n"
    "ds_read_b64_tr_b16 v[246:247], v17 offset:2048\n"
    "ds_read_b64_tr_b16 v[248:249], v18 offset:0\n"
    "ds_read_b64_tr_b16 v[250:251], v18 offset:2048\n"
    "ds_read_b64_tr_b16 v[252:253], v19 offset:0\n"
    "ds_read_b64_tr_b16 v[254:255], v19 offset:2048\n"
    "v_cmp_ne_u64 vcc, s[96:97], v[6:7]\n"
    "s_cbranch_vccnz .Ls4_ps101\n"
    ".Ls4_pb102:\n"
    "s_lshl4_add_u32 s89, s15, 0\n"
    "s_lshl_b32 s91, s15, 11\n"
    "s_lshl_b32 s90, s14, 6\n"
    "s_add_u32 s90, s90, s89\n"
    "s_lshl_b32 s90, s90, 12\n"
    "s_lshl_b32 s92, s13, 7\n"
    "s_add_u32 s90, s90, s92\n"
    "s_add_u32 m0, s91, 69632\n"
    "s_add_u32 s90, s90, 7340032\n"
    "buffer_load_dwordx4 v20, s[24:27], s90 offen sc1 lds\n"
    "buffer_load_dwordx4 v21, s[24:27], s90 offen offset:1024 sc1 lds\n"
    "s_waitcnt lgkmcnt(0)\n"
    "ds_read_b32 v3, v9 offset:64\n"
    "s_mul_i32 s90, s13, 180224\n"
    "s_add_u32 s90, s90, 9437184\n"
    "s_cmp_eq_u32 s15, 0\n"
    "s_cselect_b32 s89, 1, 2\n"
    "v_min_u32 v10, s89, v12\n"
    "v_lshlrev_b32 v10, 7, v10\n"
    "s_lshl1_add_u32 s89, s15, -1\n"
    "s_max_i32 s89, s89, 0\n"
    "s_lshl_b32 s89, s89, 7\n"
    "s_lshl_b32 s85, s13, 12\n"
    "s_add_u32 s85, s85, s89\n"
    "s_add_u32 s85, s85, 327680\n"
    "s_cmp_lt_u32 s48, 43\n"
    "s_cselect_b32 s89, s90, 0x40000000\n"
    "s_lshl_b32 s48, s48, 10\n"
    "s_add_u32 s48, s48, s89\n"
    "s_cmp_lt_u32 s49, 43\n"
    "s_cselect_b32 s89, s90, 0x40000000\n"
    "s_lshl_b32 s49, s49, 10\n"
    "s_add_u32 s49, s49, s89\n"
    "s_cmp_lt_u32 s50, 43\n"
    "s_cselect_b32 s89, s90, 0x40000000\n"
    "s_lshl_b32 s50, s50, 10\n"
    "s_add_u32 s50, s50, s89\n"
    "s_cmp_lt_u32 s51, 43\n"
    "s_cselect_b32 s89, s90, 0x40000000\n"
    "s_lshl_b32 s51, s51, 10\n"
    "s_add_u32 s51, s51, s89\n"
    "s_cmp_lt_u32 s52, 43\n"
    "s_cselect_b32 s89, s90, 0x40000000\n"
    "s_lshl_b32 s52, s52, 10\n"
    "s_add_u32 s52, s52, s89\n"
    "s_cmp_lt_u32 s53, 43\n"
    "s_cselect_b32 s89, s90, 0x40000000\n"
    "s_lshl_b32 s53, s53, 10\n"
    "s_add_u32 s53, s53, s89\n"
    "s_cmp_lt_u32 s54, 43\n"
    "s_cselect_b32 s89, s90, 0x40000000\n"
    "s_lshl_b32 s54, s54, 10\n"
    "s_add_u32 s54, s54, s89\n"
    "s_cmp_lt_u32 s55, 43\n"
    "s_cselect_b32 s89, s90, 0x40000000\n"
    "s_lshl_b32 s55, s55, 10\n"
    "s_add_u32 s55, s55, s89\n"
    "s_cmp_lt_u32 s56, 43\n"
    "s_cselect_b32 s89, s90, 0x40000000\n"
    "s_lshl_b32 s56, s56, 10\n"
    "s_add_u32 s56, s56, s89\n"
    "s_cmp_lt_u32 s57, 43\n"
    "s_cselect_b32 s89, s90, 0x40000000\n"
    "s_lshl_b32 s57, s57, 10\n"
    "s_add_u32 s57, s57, s89\n"
    "s_cmp_lt_u32 s58, 43\n"
    "s_cselect_b32 s89, s90, 0x40000000\n"
    "s_lshl_b32 s58, s58, 10\n"
    "s_add_u32 s58, s58, s89\n"
    "v_xor_b32 v16, v14, v13\n"
    "v_lshlrev_b32 v16, 4, v16\n"
    "v_lshl_add_u32 v16, v13, 8, v16\n"
    "v_add_u32 v16, s83, v16\n"
    "v_xor_b32 v17, 64, v16\n"
    "v_xor_b32 v18, 128, v16\n"
    "v_xor_b32 v19, 192, v16\n"
    "s_waitcnt lgkmcnt(0)\n"
    "v_cmp_gt_u32 vcc, 4, v3\n"
    "s_cbranch_vccnz .Ls4_ns103\n"
    ".Ls4_nb104:\n"
    "buffer_load_dwordx2 v[6:7], v10, s[24:27], s85 offen sc1\n"
    "s_waitcnt vmcnt(0)\n"
    "v_cmp_ne_u64 vcc, s[96:97], v[6:7]\n"
    "s_cbranch_vccnz .Ls4_ps106\n"
    ".Ls4_pb107:\n"
    "buffer_load_dwordx4 v[36:39], v0, s[24:27], s48 offen sc1\n"
    "buffer_load_dwordx4 v[40:43], v0, s[24:27], s49 offen sc1\n"
    "buffer_load_dwordx4 v[44:47], v0, s[24:27], s50 offen sc1\n"
    "buffer_load_dwordx4 v[48:51], v0, s[24:27], s51 offen sc1\n"
    "ds_read_b32 v3, v9 offset:96\n"
    "buffer_load_dwordx4 v[52:55], v0, s[24:27], s52 offen sc1\n"
    "s_add_u32 s85, s85, 1024\n"
    "s_waitcnt lgkmcnt(0)\n"
    "v_cmp_gt_u32 vcc, 4, v3\n"
    "s_nop 0\n"
    "s_cmp_eq_u64 vcc, 0\n"
    "s_cselect_b32 s87, s85, 0x40000000\n"
    "buffer_load_dwordx2 v[6:7], v10, s[24:27], s87 offen sc1\n"
    "buffer_load_dwordx4 v[56:59], v0, s[24:27], s53 offen sc1\n"
    "buffer_load_dwordx4 v[60:63], v0, s[24:27], s54 offen sc1\n"
    "s_waitcnt vmcnt(7)\n"
    "v_mfma_f32_16x16x32_bf16 v[20:23], v[80:83], v[36:39], 0\n"
    "v_mfma_f32_16x16x32_bf16 v[24:27], v[84:87], v[36:39], 0\n"
    "v_mfma_f32_16x16x32_bf16 v[28:31], v[88:91], v[36:39], 0\n"
    "v_mfma_f32_16x16x32_bf16 v[32:35], v[92:95], v[36:39], 0\n"
    "buffer_load_dwordx4 v[64:67], v0, s[24:27], s55 offen sc1\n"
    "s_waitcnt vmcnt(7)\n"
    "v_mfma_f32_16x16x32_bf16 v[20:23], v[96:99], v[40:43], v[20:23]\n"
    "v_mfma_f32_16x16x32_bf16 v[24:27], v[100:103], v[40:43], v[24:27]\n"
    "v_mfma_f32_16x16x32_bf16 v[28:31], v[104:107], v[40:43], v[28:31]\n"
    "v_mfma_f32_16x16x32_bf16 v[32:35], v[108:111], v[40:43], v[32:35]\n"
    "buffer_load_dwordx4 v[68:71], v0, s[24:27], s56 offen sc1\n"
    "s_waitcnt vmcnt(7)\n"
    "v_mfma_f32_16x16x32_bf16 v[20:23], v[112:115], v[44:47], v[20:23]\n"
    "v_mfma_f32_16x16x32_bf16 v[24:27], v[116:119], v[44:47], v[24:27]\n"
    "v_mfma_f32_16x16x32_bf16 v[28:31], v[120:123], v[44:47], v[28:31]\n"
    "v_mfma_f32_16x16x32_bf16 v[32:35], v[124:127], v[44:47], v[32:35]\n"
    "buffer_load_dwordx4 v[72:75], v0, s[24:27], s57 offen sc1\n"
    "s_waitcnt vmcnt(7)\n"
    "v_mfma_f32_16x16x32_bf16 v[20:23], v[128:131], v[48:51], v[20:23]\n"
    "v_mfma_f32_16x16x32_bf16 v[24:27], v[132:135], v[48:51], v[24:27]\n"
    "v_mfma_f32_16x16x32_bf16 v[28:31], v[136:139], v[48:51], v[28:31]\n"
    "v_mfma_f32_16x16x32_bf16 v[32:35], v[140:143], v[48:51], v[32:35]\n"
    "buffer_load_dwordx4 v[76:79], v0, s[24:27], s58 offen sc1\n"
    "s_waitcnt vmcnt(7)\n"
    "v_mfma_f32_16x16x32_bf16 v[20:23], v[144:147], v[52:55], v[20:23]\n"
    "v_mfma_f32_16x16x32_bf16 v[24:27], v[148:151], v[52:55], v[24:27]\n"
    "v_mfma_f32_16x16x32_bf16 v[28:31], v[152:155], v[52:55], v[28:31]\n"
    "v_mfma_f32_16x16x32_bf16 v[32:35], v[156:159], v[52:55], v[32:35]\n"
    "s_waitcnt vmcnt(6)\n"
    "v_cmp_ne_u64 vcc, s[96:97], v[6:7]\n"
    "s_cbranch_vccnz .Ls4_gn108\n"
    "s_add_u32 s48, s48, 45056\n"
    "buffer_load_dwordx4 v[36:39], v0, s[24:27], s48 offen sc1\n"
    "s_waitcnt vmcnt(6)\n"
    "v_mfma_f32_16x16x32_bf16 v[20:23], v[160:163], v[56:59], v[20:23]\n"
    "v_mfma_f32_16x16x32_bf16 v[24:27], v[164:167], v[56:59], v[24:27]\n"
    "v_mfma_f32_16x16x32_bf16 v[28:31], v[168:171], v[56:59], v[28:31]\n"
    "v_mfma_f32_16x16x32_bf16 v[32:35], v[172:175], v[56:59], v[32:35]\n"
    "s_add_u32 s49, s49, 45056\n"
    "buffer_load_dwordx4 v[40:43], v0, s[24:27], s49 offen sc1\n"
    "s_waitcnt vmcnt(6)\n"
    "v_mfma_f32_16x16x32_bf16 v[20:23], v[176:179], v[60:63], v[20:23]\n"
    "v_mfma_f32_16x16x32_bf16 v[24:27], v[180:183], v[60:63], v[24:27]\n"
    "v_mfma_f32_16x16x32_bf16 v[28:31], v[184:187], v[60:63], v[28:31]\n"
    "v_mfma_f32_16x16x32_bf16 v[32:35], v[188:191], v[60:63], v[32:35]\n"
    "s_add_u32 s50, s50, 45056\n"
    "buffer_load_dwordx4 v[44:47], v0, s[24:27], s50 offen sc1\n"
    "s_waitcnt vmcnt(6)\n"
    "v_mfma_f32_16x16x32_bf16 v[20:23], v[192:195], v[64:67], v[20:23]\n"
    "v_mfma_f32_16x16x32_bf16 v[24:27], v[196:199], v[64:67], v[24:27]\n"
    "v_mfma_f32_16x16x32_bf16 v[28:31], v[200:203], v[64:67], v[28:31]\n"
    "v_mfma_f32_16x16x32_bf16 v[32:35], v[204:207], v[64:67], v[32:35]\n"
    "s_add_u32 s51, s51, 45056\n"
    "buffer_load_dwordx4 v[48:51], v0, s[24:27], s51 offen sc1\n"
    "s_waitcnt vmcnt(6)\n"
    "v_mfma_f32_16x16x32_bf16 v[20:23], v[208:211], v[68:71], v[20:23]\n"
    "v_mfma_f32_16x16x32_bf16 v[24:27], v[212:215], v[68:71], v[24:27]\n"
    "v_mfma_f32_16x16x32_bf16 v[28:31], v[216:219], v[68:71], v[28:31]\n"
    "v_mfma_f32_16x16x32_bf16 v[32:35], v[220:223], v[68:71], v[32:35]\n"
    "ds_read_b32 v3, v9 offset:128\n"
    "s_add_u32 s52, s52, 45056\n"
    "buffer_load_dwordx4 v[52:55], v0, s[24:27], s52 offen sc1\n"
    "s_add_u32 s85, s85, 1024\n"
    "s_waitcnt lgkmcnt(0)\n"
    "v_cmp_gt_u32 vcc, 4, v3\n"
    "s_nop 0\n"
    "s_cmp_eq_u64 vcc, 0\n"
    "s_cselect_b32 s87, s85, 0x40000000\n"
    "buffer_load_dwordx2 v[6:7], v10, s[24:27], s87 offen sc1\n"
    "s_waitcnt vmcnt(7)\n"
    "v_mfma_f32_16x16x32_bf16 v[20:23], v[224:227], v[72:75], v[20:23]\n"
    "v_mfma_f32_16x16x32_bf16 v[24:27], v[228:231], v[72:75], v[24:27]\n"
    "v_mfma_f32_16x16x32_bf16 v[28:31], v[232:235], v[72:75], v[28:31]\n"
    "v_mfma_f32_16x16x32_bf16 v[32:35], v[236:239], v[72:75], v[32:35]\n"
    "s_add_u32 s53, s53, 45056\n"
    "buffer_load_dwordx4 v[56:59], v0, s[24:27], s53 offen sc1\n"
    "s_waitcnt vmcnt(7)\n"
    "v_mfma_f32_16x16x32_bf16 v[20:23], v[240:243], v[76:79], v[20:23]\n"
    "v_mfma_f32_16x16x32_bf16 v[24:27], v[244:247], v[76:79], v[24:27]\n"
    "v_mfma_f32_16x16x32_bf16 v[28:31], v[248:251], v[76:79], v[28:31]\n"
    "v_mfma_f32_16x16x32_bf16 v[32:35], v[252:255], v[76:79], v[32:35]\n"
    "s_add_u32 s54, s54, 45056\n"
    "buffer_load_dwordx4 v[60:63], v0, s[24:27], s54 offen sc1\n"
    "s_nop 2\n"
    "ds_write_b128 v16, v[20:23] offset:0\n"
    "ds_write_b128 v17, v[24:27] offset:0\n"
    "ds_write_b128 v18, v[28:31] offset:0\n"
    "ds_write_b128 v19, v[32:35] offset:0\n"
    "ds_inc_u32 v2, v2\n"
    "s_branch .Ls4_gj109\n"
    ".Ls4_gn108:\n"
    "s_waitcnt vmcnt(5)\n"
    "v_mfma_f32_16x16x32_bf16 v[20:23], v[160:163], v[56:59], v[20:23]\n"
    "v_mfma_f32_16x16x32_bf16 v[24:27], v[164:167], v[56:59], v[24:27]\n"
    "v_mfma_f32_16x16x32_bf16 v[28:31], v[168:171], v[56:59], v[28:31]\n"
    "v_mfma_f32_16x16x32_bf16 v[32:35], v[172:175], v[56:59], v[32:35]\n"
    "s_waitcnt vmcnt(4)\n"
    "v_mfma_f32_16x16x32_bf16 v[20:23], v[176:179], v[60:63], v[20:23]\n"
    "v_mfma_f32_16x16x32_bf16 v[24:27], v[180:183], v[60:63], v[24:27]\n"
    "v_mfma_f32_16x16x32_bf16 v[28:31], v[184:187], v[60:63], v[28:31]\n"
    "v_mfma_f32_16x16x32_bf16 v[32:35], v[188:191], v[60:63], v[32:35]\n"
    "s_waitcnt vmcnt(3)\n"
    "v_mfma_f32_16x16x32_bf16 v[20:23], v[192:195], v[64:67], v[20:23]\n"
    "v_mfma_f32_16x16x32_bf16 v[24:27], v[196:199], v[64:67], v[24:27]\n"
    "v_mfma_f32_16x16x32_bf16 v[28:31], v[200:203], v[64:67], v[28:31]\n"
    "v_mfma_f32_16x16x32_bf16 v[32:35], v[204:207], v[64:67], v[32:35]\n"
    "s_waitcnt vmcnt(2)\n"
    "v_mfma_f32_16x16x32_bf16 v[20:23], v[208:211], v[68:71], v[20:23]\n"
    "v_mfma_f32_16x16x32_bf16 v[24:27], v[212:215], v[68:71], v[24:27]\n"
    "v_mfma_f32_16x16x32_bf16 v[28:31], v[216:219], v[68:71], v[28:31]\n"
    "v_mfma_f32_16x16x32_bf16 v[32:35], v[220:223], v[68:71], v[32:35]\n"
    "s_waitcnt vmcnt(1)\n"
    "v_mfma_f32_16x16x32_bf16 v[20:23], v[224:227], v[72:75], v[20:23]\n"
    "v_mfma_f32_16x16x32_bf16 v[24:27], v[228:231], v[72:75], v[24:27]\n"
    "v_mfma_f32_16x16x32_bf16 v[28:31], v[232:235], v[72:75], v[28:31]\n"
    "v_mfma_f32_16x16x32_bf16 v[32:35], v[236:239], v[72:75], v[32:35]\n"
    "s_waitcnt vmcnt(0)\n"
    "v_mfma_f32_16x16x32_bf16 v[20:23], v[240:243], v[76:79], v[20:23]\n"
    "v_mfma_f32_16x16x32_bf16 v[24:27], v[244:247], v[76:79], v[24:27]\n"
    "v_mfma_f32_16x16x32_bf16 v[28:31], v[248:251], v[76:79], v[28:31]\n"
    "v_mfma_f32_16x16x32_bf16 v[32:35], v[252:255], v[76:79], v[32:35]\n"
    "ds_read_b32 v3, v9 offset:96\n"
    "s_nop 3\n"
    "ds_write_b128 v16, v[20:23] offset:0\n"
    "ds_write_b128 v17, v[24:27] offset:0\n"
    "ds_write_b128 v18, v[28:31] offset:0\n"
    "ds_write_b128 v19, v[32:35] offset:0\n"
    "ds_inc_u32 v2, v2\n"
    "s_waitcnt lgkmcnt(5)\n"
    "v_cmp_gt_u32 vcc, 4, v3\n"
    "s_cbranch_vccnz .Ls4_ns110\n"
    ".Ls4_nb111:\n"
    "buffer_load_dwordx2 v[6:7], v10, s[24:27], s85 offen sc1\n"
    "s_waitcnt vmcnt(0)\n"
    "v_cmp_ne_u64 vcc, s[96:97], v[6:7]\n"
    "s_cbranch_vccnz .Ls4_ps113\n"
    ".Ls4_pb114:\n"
    "s_add_u32 s48, s48, 45056\n"
    "buffer_load_dwordx4 v[36:39], v0, s[24:27], s48 offen sc1\n"
    "s_add_u32 s49, s49, 45056\n"
    "buffer_load_dwordx4 v[40:43], v0, s[24:27], s49 offen sc1\n"
    "s_add_u32 s50, s50, 45056\n"
    "buffer_load_dwordx4 v[44:47], v0, s[24:27], s50 offen sc1\n"
    "s_add_u32 s51, s51, 45056\n"
    "buffer_load_dwordx4 v[48:51], v0, s[24:27], s51 offen sc1\n"
    "ds_read_b32 v3, v9 offset:128\n"
    "s_add_u32 s52, s52, 45056\n"
    "buffer_load_dwordx4 v[52:55], v0, s[24:27], s52 offen sc1\n"
    "s_add_u32 s85, s85, 1024\n"
    "s_waitcnt lgkmcnt(0)\n"
    "v_cmp_gt_u32 vcc, 4, v3\n"
    "s_nop 0\n"
    "s_cmp_eq_u64 vcc, 0\n"
    "s_cselect_b32 s87, s85, 0x40000000\n"
    "buffer_load_dwordx2 v[6:7], v10, s[24:27], s87 offen sc1\n"
    "s_add_u32 s53, s53, 45056\n"
    "buffer_load_dwordx4 v[56:59], v0, s[24:27], s53 offen sc1\n"
    "s_add_u32 s54, s54, 45056\n"
    "buffer_load_dwordx4 v[60:63], v0, s[24:27], s54 offen sc1\n"
    ".Ls4_gj109:\n"
    "s_waitcnt vmcnt(7)\n"
    "v_mfma_f32_16x16x32_bf16 v[20:23], v[80:83], v[36:39], 0\n"
    "v_mfma_f32_16x16x32_bf16 v[24:27], v[84:87], v[36:39], 0\n"
    "v_mfma_f32_16x16x32_bf16 v[28:31], v[88:91], v[36:39], 0\n"
    "v_mfma_f32_16x16x32_bf16 v[32:35], v[92:95], v[36:39], 0\n"
    "s_add_u32 s55, s55, 45056\n"
    "buffer_load_dwordx4 v[64:67], v0, s[24:27], s55 offen sc1\n"
    "s_waitcnt vmcnt(7)\n"
    "v_mfma_f32_16x16x32_bf16 v[20:23], v[96:99], v[40:43], v[20:23]\n"
    "v_mfma_f32_16x16x32_bf16 v[24:27], v[100:103], v[40:43], v[24:27]\n"
    "v_mfma_f32_16x16x32_bf16 v[28:31], v[104:107], v[40:43], v[28:31]\n"
    "v_mfma_f32_16x16x32_bf16 v[32:35], v[108:111], v[40:43], v[32:35]\n"
    "s_add_u32 s56, s56, 45056\n"
    "buffer_load_dwordx4 v[68:71], v0, s[24:27], s56 offen sc1\n"
    "s_waitcnt vmcnt(7)\n"
    "v_mfma_f32_16x16x32_bf16 v[20:23], v[112:115], v[44:47], v[20:23]\n"
    "v_mfma_f32_16x16x32_bf16 v[24:27], v[116:119], v[44:47], v[24:27]\n"
    "v_mfma_f32_16x16x32_bf16 v[28:31], v[120:123], v[44:47], v[28:31]\n"
    "v_mfma_f32_16x16x32_bf16 v[32:35], v[124:127], v[44:47], v[32:35]\n"
    "s_add_u32 s57, s57, 45056\n"
    "buffer_load_dwordx4 v[72:75], v0, s[24:27], s57 offen sc1\n"
    "s_waitcnt vmcnt(7)\n"
    "v_mfma_f32_16x16x32_bf16 v[20:23], v[128:131], v[48:51], v[20:23]\n"
    "v_mfma_f32_16x16x32_bf16 v[24:27], v[132:135], v[48:51], v[24:27]\n"
    "v_mfma_f32_16x16x32_bf16 v[28:31], v[136:139], v[48:51], v[28:31]\n"
    "v_mfma_f32_16x16x32_bf16 v[32:35], v[140:143], v[48:51], v[32:35]\n"
    "s_add_u32 s58, s58, 45056\n"
    "buffer_load_dwordx4 v[76:79], v0, s[24:27], s58 offen sc1\n"
    "s_waitcnt vmcnt(7)\n"
    "v_mfma_f32_16x16x32_bf16 v[20:23], v[144:147], v[52:55], v[20:23]\n"
    "v_mfma_f32_16x16x32_bf16 v[24:27], v[148:151], v[52:55], v[24:27]\n"
    "v_mfma_f32_16x16x32_bf16 v[28:31], v[152:155], v[52:55], v[28:31]\n"
    "v_mfma_f32_16x16x32_bf16 v[32:35], v[156:159], v[52:55], v[32:35]\n"
    "s_waitcnt vmcnt(6)\n"
    "v_cmp_ne_u64 vcc, s[96:97], v[6:7]\n"
    "s_cbranch_vccnz .Ls4_gn115\n"
    "s_add_u32 s48, s48, 45056\n"
    "buffer_load_dwordx4 v[36:39], v0, s[24:27], s48 offen sc1\n"
    "s_waitcnt vmcnt(6)\n"
    "v_mfma_f32_16x16x32_bf16 v[20:23], v[160:163], v[56:59], v[20:23]\n"
    "v_mfma_f32_16x16x32_bf16 v[24:27], v[164:167], v[56:59], v[24:27]\n"
    "v_mfma_f32_16x16x32_bf16 v[28:31], v[168:171], v[56:59], v[28:31]\n"
    "v_mfma_f32_16x16x32_bf16 v[32:35], v[172:175], v[56:59], v[32:35]\n"
    "s_add_u32 s49, s49, 45056\n"
    "buffer_load_dwordx4 v[40:43], v0, s[24:27], s49 offen sc1\n"
    "s_waitcnt vmcnt(6)\n"
    "v_mfma_f32_16x16x32_bf16 v[20:23], v[176:179], v[60:63], v[20:23]\n"
    "v_mfma_f32_16x16x32_bf16 v[24:27], v[180:183], v[60:63], v[24:27]\n"
    "v_mfma_f32_16x16x32_bf16 v[28:31], v[184:187], v[60:63], v[28:31]\n"
    "v_mfma_f32_16x16x32_bf16 v[32:35], v[188:191], v[60:63], v[32:35]\n"
    "s_add_u32 s50, s50, 45056\n"
    "buffer_load_dwordx4 v[44:47], v0, s[24:27], s50 offen sc1\n"
    "s_waitcnt vmcnt(6)\n"
    "v_mfma_f32_16x16x32_bf16 v[20:23], v[192:195], v[64:67], v[20:23]\n"
    "v_mfma_f32_16x16x32_bf16 v[24:27], v[196:199], v[64:67], v[24:27]\n"
    "v_mfma_f32_16x16x32_bf16 v[28:31], v[200:203], v[64:67], v[28:31]\n"
    "v_mfma_f32_16x16x32_bf16 v[32:35], v[204:207], v[64:67], v[32:35]\n"
    "s_add_u32 s51, s51, 45056\n"
    "buffer_load_dwordx4 v[48:51], v0, s[24:27], s51 offen sc1\n"
    "s_waitcnt vmcnt(6)\n"
    "v_mfma_f32_16x16x32_bf16 v[20:23], v[208:211], v[68:71], v[20:23]\n"
    "v_mfma_f32_16x16x32_bf16 v[24:27], v[212:215], v[68:71], v[24:27]\n"
    "v_mfma_f32_16x16x32_bf16 v[28:31], v[216:219], v[68:71], v[28:31]\n"
    "v_mfma_f32_16x16x32_bf16 v[32:35], v[220:223], v[68:71], v[32:35]\n"
    "ds_read_b32 v3, v9 offset:160\n"
    "s_add_u32 s52, s52, 45056\n"
    "buffer_load_dwordx4 v[52:55], v0, s[24:27], s52 offen sc1\n"
    "s_add_u32 s85, s85, 1024\n"
    "s_waitcnt lgkmcnt(0)\n"
    "v_cmp_gt_u32 vcc, 4, v3\n"
    "s_nop 0\n"
    "s_cmp_eq_u64 vcc, 0\n"
    "s_cselect_b32 s87, s85, 0x40000000\n"
    "buffer_load_dwordx2 v[6:7], v10, s[24:27], s87 offen sc1\n"
    "s_waitcnt vmcnt(7)\n"
    "v_mfma_f32_16x16x32_bf16 v[20:23], v[224:227], v[72:75], v[20:23]\n"
    "v_mfma_f32_16x16x32_bf16 v[24:27], v[228:231], v[72:75], v[24:27]\n"
    "v_mfma_f32_16x16x32_bf16 v[28:31], v[232:235], v[72:75], v[28:31]\n"
    "v_mfma_f32_16x16x32_bf16 v[32:35], v[236:239], v[72:75], v[32:35]\n"
    "s_add_u32 s53, s53, 45056\n"
    "buffer_load_dwordx4 v[56:59], v0, s[24:27], s53 offen sc1\n"
    "s_waitcnt vmcnt(7)\n"
    "v_mfma_f32_16x16x32_bf16 v[20:23], v[240:243], v[76:79], v[20:23]\n"
    "v_mfma_f32_16x16x32_bf16 v[24:27], v[244:247], v[76:79], v[24:27]\n"
    "v_mfma_f32_16x16x32_bf16 v[28:31], v[248:251], v[76:79], v[28:31]\n"
    "v_mfma_f32_16x16x32_bf16 v[32:35], v[252:255], v[76:79], v[32:35]\n"
    "s_add_u32 s54, s54, 45056\n"
    "buffer_load_dwordx4 v[60:63], v0, s[24:27], s54 offen sc1\n"
    "s_nop 2\n"
    "ds_write_b128 v16, v[20:23] offset:16384\n"
    "ds_write_b128 v17, v[24:27] offset:16384\n"
    "ds_write_b128 v18, v[28:31] offset:16384\n"
    "ds_write_b128 v19, v[32:35] offset:16384\n"
    "ds_inc_u32 v2, v2\n"
    "s_branch .Ls4_gj116\n"
    ".Ls4_gn115:\n"
    "s_waitcnt vmcnt(5)\n"
    "v_mfma_f32_16x16x32_bf16 v[20:23], v[160:163], v[56:59], v[20:23]\n"
    "v_mfma_f32_16x16x32_bf16 v[24:27], v[164:167], v[56:59], v[24:27]\n"
    "v_mfma_f32_16x16x32_bf16 v[28:31], v[168:171], v[56:59], v[28:31]\n"
    "v_mfma_f32_16x16x32_bf16 v[32:35], v[172:175], v[56:59], v[32:35]\n"
    "s_waitcnt vmcnt(4)\n"
    "v_mfma_f32_16x16x32_bf16 v[20:23], v[176:179], v[60:63], v[20:23]\n"
    "v_mfma_f32_16x16x32_bf16 v[24:27], v[180:183], v[60:63], v[24:27]\n"
    "v_mfma_f32_16x16x32_bf16 v[28:31], v[184:187], v[60:63], v[28:31]\n"
    "v_mfma_f32_16x16x32_bf16 v[32:35], v[188:191], v[60:63], v[32:35]\n"
    "s_waitcnt vmcnt(3)\n"
    "v_mfma_f32_16x16x32_bf16 v[20:23], v[192:195], v[64:67], v[20:23]\n"
    "v_mfma_f32_16x16x32_bf16 v[24:27], v[196:199], v[64:67], v[24:27]\n"
    "v_mfma_f32_16x16x32_bf16 v[28:31], v[200:203], v[64:67], v[28:31]\n"
    "v_mfma_f32_16x16x32_bf16 v[32:35], v[204:207], v[64:67], v[32:35]\n"
    "s_waitcnt vmcnt(2)\n"
    "v_mfma_f32_16x16x32_bf16 v[20:23], v[208:211], v[68:71], v[20:23]\n"
    "v_mfma_f32_16x16x32_bf16 v[24:27], v[212:215], v[68:71], v[24:27]\n"
    "v_mfma_f32_16x16x32_bf16 v[28:31], v[216:219], v[68:71], v[28:31]\n"
    "v_mfma_f32_16x16x32_bf16 v[32:35], v[220:223], v[68:71], v[32:35]\n"
    "s_waitcnt vmcnt(1)\n"
    "v_mfma_f32_16x16x32_bf16 v[20:23], v[224:227], v[72:75], v[20:23]\n"
    "v_mfma_f32_16x16x32_bf16 v[24:27], v[228:231], v[72:75], v[24:27]\n"
    "v_mfma_f32_16x16x32_bf16 v[28:31], v[232:235], v[72:75], v[28:31]\n"
    "v_mfma_f32_16x16x32_bf16 v[32:35], v[236:239], v[72:75], v[32:35]\n"
    "s_waitcnt vmcnt(0)\n"
    "v_mfma_f32_16x16x32_bf16 v[20:23], v[240:243], v[76:79], v[20:23]\n"
    "v_mfma_f32_16x16x32_bf16 v[24:27], v[244:247], v[76:79], v[24:27]\n"
    "v_mfma_f32_16x16x32_bf16 v[28:31], v[248:251], v[76:79], v[28:31]\n"
    "v_mfma_f32_16x16x32_bf16 v[32:35], v[252:255], v[76:79], v[32:35]\n"
    "ds_read_b32 v3, v9 offset:128\n"
    "s_nop 3\n"
    "ds_write_b128 v16, v[20:23] offset:16384\n"
    "ds_write_b128 v17, v[24:27] offset:16384\n"
    "ds_write_b128 v18, v[28:31] offset:16384\n"
    "ds_write_b128 v19, v[32:35] offset:16384\n"
    "ds_inc_u32 v2, v2\n"
    "s_waitcnt lgkmcnt(5)\n"
    "v_cmp_gt_u32 vcc, 4, v3\n"
    "s_cbranch_vccnz .Ls4_ns117\n"
    ".Ls4_nb118:\n"
    "buffer_load_dwordx2 v[6:7], v10, s[24:27], s85 offen sc1\n"
    "s_waitcnt vmcnt(0)\n"
    "v_cmp_ne_u64 vcc, s[96:97], v[6:7]\n"
    "s_cbranch_vccnz .Ls4_ps120\n"
    ".Ls4_pb121:\n"
    "s_add_u32 s48, s48, 45056\n"
    "buffer_load_dwordx4 v[36:39], v0, s[24:27], s48 offen sc1\n"
    "s_add_u32 s49, s49, 45056\n"
    "buffer_load_dwordx4 v[40:43], v0, s[24:27], s49 offen sc1\n"
    "s_add_u32 s50, s50, 45056\n"
    "buffer_load_dwordx4 v[44:47], v0, s[24:27], s50 offen sc1\n"
    "s_add_u32 s51, s51, 45056\n"
    "buffer_load_dwordx4 v[48:51], v0, s[24:27], s51 offen sc1\n"
    "ds_read_b32 v3, v9 offset:160\n"
    "s_add_u32 s52, s52, 45056\n"
    "buffer_load_dwordx4 v[52:55], v0, s[24:27], s52 offen sc1\n"
    "s_add_u32 s85, s85, 1024\n"
    "s_waitcnt lgkmcnt(0)\n"
    "v_cmp_gt_u32 vcc, 4, v3\n"
    "s_nop 0\n"
    "s_cmp_eq_u64 vcc, 0\n"
    "s_cselect_b32 s87, s85, 0x40000000\n"
    "buffer_load_dwordx2 v[6:7], v10, s[24:27], s87 offen sc1\n"
    "s_add_u32 s53, s53, 45056\n"
    "buffer_load_dwordx4 v[56:59], v0, s[24:27], s53 offen sc1\n"
    "s_add_u32 s54, s54, 45056\n"
    "buffer_load_dwordx4 v[60:63], v0, s[24:27], s54 offen sc1\n"
    ".Ls4_gj116:\n"
    "s_waitcnt vmcnt(7)\n"
    "v_mfma_f32_16x16x32_bf16 v[20:23], v[80:83], v[36:39], 0\n"
    "v_mfma_f32_16x16x32_bf16 v[24:27], v[84:87], v[36:39], 0\n"
    "v_mfma_f32_16x16x32_bf16 v[28:31], v[88:91], v[36:39], 0\n"
    "v_mfma_f32_16x16x32_bf16 v[32:35], v[92:95], v[36:39], 0\n"
    "s_add_u32 s55, s55, 45056\n"
    "buffer_load_dwordx4 v[64:67], v0, s[24:27], s55 offen sc1\n"
    "s_waitcnt vmcnt(7)\n"
    "v_mfma_f32_16x16x32_bf16 v[20:23], v[96:99], v[40:43], v[20:23]\n"
    "v_mfma_f32_16x16x32_bf16 v[24:27], v[100:103], v[40:43], v[24:27]\n"
    "v_mfma_f32_16x16x32_bf16 v[28:31], v[104:107], v[40:43], v[28:31]\n"
    "v_mfma_f32_16x16x32_bf16 v[32:35], v[108:111], v[40:43], v[32:35]\n"
    "s_add_u32 s56, s56, 45056\n"
    "buffer_load_dwordx4 v[68:71], v0, s[24:27], s56 offen sc1\n"
    "s_waitcnt vmcnt(7)\n"
    "v_mfma_f32_16x16x32_bf16 v[20:23], v[112:115], v[44:47], v[20:23]\n"
    "v_mfma_f32_16x16x32_bf16 v[24:27], v[116:119], v[44:47], v[24:27]\n"
    "v_mfma_f32_16x16x32_bf16 v[28:31], v[120:123], v[44:47], v[28:31]\n"
    "v_mfma_f32_16x16x32_bf16 v[32:35], v[124:127], v[44:47], v[32:35]\n"
    "s_add_u32 s57, s57, 45056\n"
    "buffer_load_dwordx4 v[72:75], v0, s[24:27], s57 offen sc1\n"
    "s_waitcnt vmcnt(7)\n"
    "v_mfma_f32_16x16x32_bf16 v[20:23], v[128:131], v[48:51], v[20:23]\n"
    "v_mfma_f32_16x16x32_bf16 v[24:27], v[132:135], v[48:51], v[24:27]\n"
    "v_mfma_f32_16x16x32_bf16 v[28:31], v[136:139], v[48:51], v[28:31]\n"
    "v_mfma_f32_16x16x32_bf16 v[32:35], v[140:143], v[48:51], v[32:35]\n"
    "s_add_u32 s58, s58, 45056\n"
    "buffer_load_dwordx4 v[76:79], v0, s[24:27], s58 offen sc1\n"
    "s_waitcnt vmcnt(7)\n"
    "v_mfma_f32_16x16x32_bf16 v[20:23], v[144:147], v[52:55], v[20:23]\n"
    "v_mfma_f32_16x16x32_bf16 v[24:27], v[148:151], v[52:55], v[24:27]\n"
    "v_mfma_f32_16x16x32_bf16 v[28:31], v[152:155], v[52:55], v[28:31]\n"
    "v_mfma_f32_16x16x32_bf16 v[32:35], v[156:159], v[52:55], v[32:35]\n"
    "s_waitcnt vmcnt(6)\n"
    "v_cmp_ne_u64 vcc, s[96:97], v[6:7]\n"
    "s_cbranch_vccnz .Ls4_gn122\n"
    "s_add_u32 s48, s48, 45056\n"
    "buffer_load_dwordx4 v[36:39], v0, s[24:27], s48 offen sc1\n"
    "s_waitcnt vmcnt(6)\n"
    "v_mfma_f32_16x16x32_bf16 v[20:23], v[160:163], v[56:59], v[20:23]\n"
    "v_mfma_f32_16x16x32_bf16 v[24:27], v[164:167], v[56:59], v[24:27]\n"
    "v_mfma_f32_16x16x32_bf16 v[28:31], v[168:171], v[56:59], v[28:31]\n"
    "v_mfma_f32_16x16x32_bf16 v[32:35], v[172:175], v[56:59], v[32:35]\n"
    "s_add_u32 s49, s49, 45056\n"
    "buffer_load_dwordx4 v[40:43], v0, s[24:27], s49 offen sc1\n"
    "s_waitcnt vmcnt(6)\n"
    "v_mfma_f32_16x16x32_bf16 v[20:23], v[176:179], v[60:63], v[20:23]\n"
    "v_mfma_f32_16x16x32_bf16 v[24:27], v[180:183], v[60:63], v[24:27]\n"
    "v_mfma_f32_16x16x32_bf16 v[28:31], v[184:187], v[60:63], v[28:31]\n"
    "v_mfma_f32_16x16x32_bf16 v[32:35], v[188:191], v[60:63], v[32:35]\n"
    "s_add_u32 s50, s50, 45056\n"
    "buffer_load_dwordx4 v[44:47], v0, s[24:27], s50 offen sc1\n"
    "s_waitcnt vmcnt(6)\n"
    "v_mfma_f32_16x16x32_bf16 v[20:23], v[192:195], v[64:67], v[20:23]\n"
    "v_mfma_f32_16x16x32_bf16 v[24:27], v[196:199], v[64:67], v[24:27]\n"
    "v_mfma_f32_16x16x32_bf16 v[28:31], v[200:203], v[64:67], v[28:31]\n"
    "v_mfma_f32_16x16x32_bf16 v[32:35], v[204:207], v[64:67], v[32:35]\n"
    "s_add_u32 s51, s51, 45056\n"
    "buffer_load_dwordx4 v[48:51], v0, s[24:27], s51 offen sc1\n"
    "s_waitcnt vmcnt(6)\n"
    "v_mfma_f32_16x16x32_bf16 v[20:23], v[208:211], v[68:71], v[20:23]\n"
    "v_mfma_f32_16x16x32_bf16 v[24:27], v[212:215], v[68:71], v[24:27]\n"
    "v_mfma_f32_16x16x32_bf16 v[28:31], v[216:219], v[68:71], v[28:31]\n"
    "v_mfma_f32_16x16x32_bf16 v[32:35], v[220:223], v[68:71], v[32:35]\n"
    "s_add_u32 s52, s52, 45056\n"
    "buffer_load_dwordx4 v[52:55], v0, s[24:27], s52 offen sc1\n"
    "s_waitcnt vmcnt(6)\n"
    "v_mfma_f32_16x16x32_bf16 v[20:23], v[224:227], v[72:75], v[20:23]\n"
    "v_mfma_f32_16x16x32_bf16 v[24:27], v[228:231], v[72:75], v[24:27]\n"
    "v_mfma_f32_16x16x32_bf16 v[28:31], v[232:235], v[72:75], v[28:31]\n"
    "v_mfma_f32_16x16x32_bf16 v[32:35], v[236:239], v[72:75], v[32:35]\n"
    "s_add_u32 s53, s53, 45056\n"
    "buffer_load_dwordx4 v[56:59], v0, s[24:27], s53 offen sc1\n"
    "s_waitcnt vmcnt(6)\n"
    "v_mfma_f32_16x16x32_bf16 v[20:23], v[240:243], v[76:79], v[20:23]\n"
    "v_mfma_f32_16x16x32_bf16 v[24:27], v[244:247], v[76:79], v[24:27]\n"
    "v_mfma_f32_16x16x32_bf16 v[28:31], v[248:251], v[76:79], v[28:31]\n"
    "v_mfma_f32_16x16x32_bf16 v[32:35], v[252:255], v[76:79], v[32:35]\n"
    "s_add_u32 s54, s54, 45056\n"
    "buffer_load_dwordx4 v[60:63], v0, s[24:27], s54 offen sc1\n"
    "s_nop 2\n"
    "ds_write_b128 v16, v[20:23] offset:32768\n"
    "ds_write_b128 v17, v[24:27] offset:32768\n"
    "ds_write_b128 v18, v[28:31] offset:32768\n"
    "ds_write_b128 v19, v[32:35] offset:32768\n"
    "ds_inc_u32 v2, v2\n"
    "s_branch .Ls4_gj123\n"
    ".Ls4_gn122:\n"
    "s_waitcnt vmcnt(5)\n"
    "v_mfma_f32_16x16x32_bf16 v[20:23], v[160:163], v[56:59], v[20:23]\n"
    "v_mfma_f32_16x16x32_bf16 v[24:27], v[164:167], v[56:59], v[24:27]\n"
    "v_mfma_f32_16x16x32_bf16 v[28:31], v[168:171], v[56:59], v[28:31]\n"
    "v_mfma_f32_16x16x32_bf16 v[32:35], v[172:175], v[56:59], v[32:35]\n"
    "s_waitcnt vmcnt(4)\n"
    "v_mfma_f32_16x16x32_bf16 v[20:23], v[176:179], v[60:63], v[20:23]\n"
    "v_mfma_f32_16x16x32_bf16 v[24:27], v[180:183], v[60:63], v[24:27]\n"
    "v_mfma_f32_16x16x32_bf16 v[28:31], v[184:187], v[60:63], v[28:31]\n"
    "v_mfma_f32_16x16x32_bf16 v[32:35], v[188:191], v[60:63], v[32:35]\n"
    "s_waitcnt vmcnt(3)\n"
    "v_mfma_f32_16x16x32_bf16 v[20:23], v[192:195], v[64:67], v[20:23]\n"
    "v_mfma_f32_16x16x32_bf16 v[24:27], v[196:199], v[64:67], v[24:27]\n"
    "v_mfma_f32_16x16x32_bf16 v[28:31], v[200:203], v[64:67], v[28:31]\n"
    "v_mfma_f32_16x16x32_bf16 v[32:35], v[204:207], v[64:67], v[32:35]\n"
    "s_waitcnt vmcnt(2)\n"
    "v_mfma_f32_16x16x32_bf16 v[20:23], v[208:211], v[68:71], v[20:23]\n"
    "v_mfma_f32_16x16x32_bf16 v[24:27], v[212:215], v[68:71], v[24:27]\n"
    "v_mfma_f32_16x16x32_bf16 v[28:31], v[216:219], v[68:71], v[28:31]\n"
    "v_mfma_f32_16x16x32_bf16 v[32:35], v[220:223], v[68:71], v[32:35]\n"
    "s_waitcnt vmcnt(1)\n"
    "v_mfma_f32_16x16x32_bf16 v[20:23], v[224:227], v[72:75], v[20:23]\n"
    "v_mfma_f32_16x16x32_bf16 v[24:27], v[228:231], v[72:75], v[24:27]\n"
    "v_mfma_f32_16x16x32_bf16 v[28:31], v[232:235], v[72:75], v[28:31]\n"
    "v_mfma_f32_16x16x32_bf16 v[32:35], v[236:239], v[72:75], v[32:35]\n"
    "s_waitcnt vmcnt(0)\n"
    "v_mfma_f32_16x16x32_bf16 v[20:23], v[240:243], v[76:79], v[20:23]\n"
    "v_mfma_f32_16x16x32_bf16 v[24:27], v[244:247], v[76:79], v[24:27]\n"
    "v_mfma_f32_16x16x32_bf16 v[28:31], v[248:251], v[76:79], v[28:31]\n"
    "v_mfma_f32_16x16x32_bf16 v[32:35], v[252:255], v[76:79], v[32:35]\n"
    "ds_read_b32 v3, v9 offset:160\n"
    "s_nop 3\n"
    "ds_write_b128 v16, v[20:23] offset:32768\n"
    "ds_write_b128 v17, v[24:27] offset:32768\n"
    "ds_write_b128 v18, v[28:31] offset:32768\n"
    "ds_write_b128 v19, v[32:35] offset:32768\n"
    "ds_inc_u32 v2, v2\n"
    "s_waitcnt lgkmcnt(5)\n"
    "v_cmp_gt_u32 vcc, 4, v3\n"
    "s_cbranch_vccnz .Ls4_ns124\n"
    ".Ls4_nb125:\n"
    "buffer_load_dwordx2 v[6:7], v10, s[24:27], s85 offen sc1\n"
    "s_waitcnt vmcnt(0)\n"
    "v_cmp_ne_u64 vcc, s[96:97], v[6:7]\n"
    "s_cbranch_vccnz .Ls4_ps127\n"
    ".Ls4_pb128:\n"
    "s_add_u32 s48, s48, 45056\n"
    "buffer_load_dwordx4 v[36:39], v0, s[24:27], s48 offen sc1\n"
    "s_add_u32 s49, s49, 45056\n"
    "buffer_load_dwordx4 v[40:43], v0, s[24:27], s49 offen sc1\n"
    "s_add_u32 s50, s50, 45056\n"
    "buffer_load_dwordx4 v[44:47], v0, s[24:27], s50 offen sc1\n"
    "s_add_u32 s51, s51, 45056\n"
    "buffer_load_dwordx4 v[48:51], v0, s[24:27], s51 offen sc1\n"
    "s_add_u32 s52, s52, 45056\n"
    "buffer_load_dwordx4 v[52:55], v0, s[24:27], s52 offen sc1\n"
    "s_add_u32 s53, s53, 45056\n"
    "buffer_load_dwordx4 v[56:59], v0, s[24:27], s53 offen sc1\n"
    "s_add_u32 s54, s54, 45056\n"
    "buffer_load_dwordx4 v[60:63], v0, s[24:27], s54 offen sc1\n"
    ".Ls4_gj123:\n"
    "s_waitcnt vmcnt(6)\n"
    "v_mfma_f32_16x16x32_bf16 v[20:23], v[80:83], v[36:39], 0\n"
    "v_mfma_f32_16x16x32_bf16 v[24:27], v[84:87], v[36:39], 0\n"
    "v_mfma_f32_16x16x32_bf16 v[28:31], v[88:91], v[36:39], 0\n"
    "v_mfma_f32_16x16x32_bf16 v[32:35], v[92:95], v[36:39], 0\n"
    "s_add_u32 s55, s55, 45056\n"
    "buffer_load_dwordx4 v[64:67], v0, s[24:27], s55 offen sc1\n"
    "s_waitcnt vmcnt(6)\n"
    "v_mfma_f32_16x16x32_bf16 v[20:23], v[96:99], v[40:43], v[20:23]\n"
    "v_mfma_f32_16x16x32_bf16 v[24:27], v[100:103], v[40:43], v[24:27]\n"
    "v_mfma_f32_16x16x32_bf16 v[28:31], v[104:107], v[40:43], v[28:31]\n"
    "v_mfma_f32_16x16x32_bf16 v[32:35], v[108:111], v[40:43], v[32:35]\n"
    "s_add_u32 s56, s56, 45056\n"
    "buffer_load_dwordx4 v[68:71], v0, s[24:27], s56 offen sc1\n"
    "s_waitcnt vmcnt(6)\n"
    "v_mfma_f32_16x16x32_bf16 v[20:23], v[112:115], v[44:47], v[20:23]\n"
    "v_mfma_f32_16x16x32_bf16 v[24:27], v[116:119], v[44:47], v[24:27]\n"
    "v_mfma_f32_16x16x32_bf16 v[28:31], v[120:123], v[44:47], v[28:31]\n"
    "v_mfma_f32_16x16x32_bf16 v[32:35], v[124:127], v[44:47], v[32:35]\n"
    "s_add_u32 s57, s57, 45056\n"
    "buffer_load_dwordx4 v[72:75], v0, s[24:27], s57 offen sc1\n"
    "s_waitcnt vmcnt(6)\n"
    "v_mfma_f32_16x16x32_bf16 v[20:23], v[128:131], v[48:51], v[20:23]\n"
    "v_mfma_f32_16x16x32_bf16 v[24:27], v[132:135], v[48:51], v[24:27]\n"
    "v_mfma_f32_16x16x32_bf16 v[28:31], v[136:139], v[48:51], v[28:31]\n"
    "v_mfma_f32_16x16x32_bf16 v[32:35], v[140:143], v[48:51], v[32:35]\n"
    "s_add_u32 s58, s58, 45056\n"
    "buffer_load_dwordx4 v[76:79], v0, s[24:27], s58 offen sc1\n"
    "s_waitcnt vmcnt(6)\n"
    "v_mfma_f32_16x16x32_bf16 v[20:23], v[144:147], v[52:55], v[20:23]\n"
    "v_mfma_f32_16x16x32_bf16 v[24:27], v[148:151], v[52:55], v[24:27]\n"
    "v_mfma_f32_16x16x32_bf16 v[28:31], v[152:155], v[52:55], v[28:31]\n"
    "v_mfma_f32_16x16x32_bf16 v[32:35], v[156:159], v[52:55], v[32:35]\n"
    "s_waitcnt vmcnt(5)\n"
    "v_mfma_f32_16x16x32_bf16 v[20:23], v[160:163], v[56:59], v[20:23]\n"
    "v_mfma_f32_16x16x32_bf16 v[24:27], v[164:167], v[56:59], v[24:27]\n"
    "v_mfma_f32_16x16x32_bf16 v[28:31], v[168:171], v[56:59], v[28:31]\n"
    "v_mfma_f32_16x16x32_bf16 v[32:35], v[172:175], v[56:59], v[32:35]\n"
    "s_waitcnt vmcnt(4)\n"
    "v_mfma_f32_16x16x32_bf16 v[20:23], v[176:179], v[60:63], v[20:23]\n"
    "v_mfma_f32_16x16x32_bf16 v[24:27], v[180:183], v[60:63], v[24:27]\n"
    "v_mfma_f32_16x16x32_bf16 v[28:31], v[184:187], v[60:63], v[28:31]\n"
    "v_mfma_f32_16x16x32_bf16 v[32:35], v[188:191], v[60:63], v[32:35]\n"
    "s_waitcnt vmcnt(3)\n"
    "v_mfma_f32_16x16x32_bf16 v[20:23], v[192:195], v[64:67], v[20:23]\n"
    "v_mfma_f32_16x16x32_bf16 v[24:27], v[196:199], v[64:67], v[24:27]\n"
    "v_mfma_f32_16x16x32_bf16 v[28:31], v[200:203], v[64:67], v[28:31]\n"
    "v_mfma_f32_16x16x32_bf16 v[32:35], v[204:207], v[64:67], v[32:35]\n"
    "s_waitcnt vmcnt(2)\n"
    "v_mfma_f32_16x16x32_bf16 v[20:23], v[208:211], v[68:71], v[20:23]\n"
    "v_mfma_f32_16x16x32_bf16 v[24:27], v[212:215], v[68:71], v[24:27]\n"
    "v_mfma_f32_16x16x32_bf16 v[28:31], v[216:219], v[68:71], v[28:31]\n"
    "v_mfma_f32_16x16x32_bf16 v[32:35], v[220:223], v[68:71], v[32:35]\n"
    "s_waitcnt vmcnt(1)\n"
    "v_mfma_f32_16x16x32_bf16 v[20:23], v[224:227], v[72:75], v[20:23]\n"
    "v_mfma_f32_16x16x32_bf16 v[24:27], v[228:231], v[72:75], v[24:27]\n"
    "v_mfma_f32_16x16x32_bf16 v[28:31], v[232:235], v[72:75], v[28:31]\n"
    "v_mfma_f32_16x16x32_bf16 v[32:35], v[236:239], v[72:75], v[32:35]\n"
    "s_waitcnt vmcnt(0)\n"
    "v_mfma_f32_16x16x32_bf16 v[20:23], v[240:243], v[76:79], v[20:23]\n"
    "v_mfma_f32_16x16x32_bf16 v[24:27], v[244:247], v[76:79], v[24:27]\n"
    "v_mfma_f32_16x16x32_bf16 v[28:31], v[248:251], v[76:79], v[28:31]\n"
    "v_mfma_f32_16x16x32_bf16 v[32:35], v[252:255], v[76:79], v[32:35]\n"
    "s_nop 4\n"
    "ds_write_b128 v16, v[20:23] offset:49152\n"
    "ds_write_b128 v17, v[24:27] offset:49152\n"
    "ds_write_b128 v18, v[28:31] offset:49152\n"
    "ds_write_b128 v19, v[32:35] offset:49152\n"
    "ds_inc_u32 v2, v2\n"
    "s_endpgm\n"
    ".Ls4_bt0:\n"
    "s_xor_b32 s93, s75, 0\n"
    "s_mul_i32 s93, s93, 88064\n"
    "s_add_u32 m0, s74, 0\n"
    "s_add_u32 s93, s93, s73\n"
    "buffer_load_dwordx4 v224, s[32:35], s93 offen offset:0 lds\n"
    "buffer_load_dwordx4 v225, s[32:35], s93 offen offset:1024 lds\n"
    "buffer_load_dwordx4 v226, s[32:35], s93 offen offset:2048 lds\n"
    "s_xor_b32 s93, s75, 1\n"
    "s_mul_i32 s93, s93, 88064\n"
    "s_add_u32 m0, s74, 12288\n"
    "s_add_u32 s93, s93, s73\n"
    "buffer_load_dwordx4 v224, s[32:35], s93 offen offset:0 lds\n"
    "buffer_load_dwordx4 v225, s[32:35], s93 offen offset:1024 lds\n"
    "s_mov_b32 s72, 2\n"
    "s_add_u32 s68, s68, .Ls4_bt1-.Ls4_bt0\n"
    "s_addc_u32 s69, s69, 0\n"
    "s_setpc_b64 s[70:71]\n"
    ".Ls4_bt1:\n"
    "s_xor_b32 s93, s75, 1\n"
    "s_mul_i32 s93, s93, 88064\n"
    "s_add_u32 m0, s74, 12288\n"
    "s_add_u32 s93, s93, s73\n"
    "buffer_load_dwordx4 v226, s[32:35], s93 offen offset:2048 lds\n"
    "s_xor_b32 s93, s75, 2\n"
    "s_mul_i32 s93, s93, 88064\n"
    "s_add_u32 m0, s74, 24576\n"
    "s_add_u32 s93, s93, s73\n"
    "buffer_load_dwordx4 v224, s[32:35], s93 offen offset:0 lds\n"
    "buffer_load_dwordx4 v225, s[32:35], s93 offen offset:1024 lds\n"
    "buffer_load_dwordx4 v226, s[32:35], s93 offen offset:2048 lds\n"
    "s_xor_b32 s93, s75, 3\n"
    "s_mul_i32 s93, s93, 88064\n"
    "s_add_u32 m0, s74, 36864\n"
    "s_add_u32 s93, s93, s73\n"
    "buffer_load_dwordx4 v224, s[32:35], s93 offen offset:0 lds\n"
    "s_mov_b32 s72, 2\n"
    "s_add_u32 s68, s68, .Ls4_bt2-.Ls4_bt1\n"
    "s_addc_u32 s69, s69, 0\n"
    "s_setpc_b64 s[70:71]\n"
    ".Ls4_bt2:\n"
    "s_xor_b32 s93, s75, 3\n"
    "s_mul_i32 s93, s93, 88064\n"
    "s_add_u32 m0, s74, 36864\n"
    "s_add_u32 s93, s93, s73\n"
    "buffer_load_dwordx4 v225, s[32:35], s93 offen offset:1024 lds\n"
    "buffer_load_dwordx4 v226, s[32:35], s93 offen offset:2048 lds\n"
    "s_xor_b32 s93, s75, 4\n"
    "s_mul_i32 s93, s93, 88064\n"
    "s_add_u32 m0, s74, 49152\n"
    "s_add_u32 s93, s93, s73\n"
    "buffer_load_dwordx4 v224, s[32:35], s93 offen offset:0 lds\n"
    "buffer_load_dwordx4 v225, s[32:35], s93 offen offset:1024 lds\n"
    "buffer_load_dwordx4 v226, s[32:35], s93 offen offset:2048 lds\n"
    "s_mov_b32 s72, 9\n"
    "s_add_u32 s68, s68, .Ls4_bt3-.Ls4_bt2\n"
    "s_addc_u32 s69, s69, 0\n"
    "s_setpc_b64 s[70:71]\n"
    ".Ls4_bt3:\n"
    "s_waitcnt vmcnt(0)\n"
    "s_mov_b64 exec, 1\n"
    "ds_inc_u32 v227, v2 offset:224\n"
    "s_mov_b64 exec, -1\n"
    "s_mov_b32 s72, 32767\n"
    "s_setpc_b64 s[70:71]\n"
    ".Ls4_ps70:\n"
    "buffer_load_dwordx2 v[6:7], v212, s[24:27], s61 offen sc1\n"
    "s_waitcnt vmcnt(0)\n"
    "v_cmp_ne_u64 vcc, s[96:97], v[6:7]\n"
    "s_cbranch_vccz .Ls4_pb71\n"
    "s_sleep 8\n"
    "s_sub_u32 s100, s100, 1\n"
    "s_cselect_b32 s100, 0, s100\n"
    "s_cbranch_scc0 .Ls4_ps70\n"
    "s_branch .Ls4_pb71\n"
    ".Ls4_cs75:\n"
    "ds_read_b32 v3, v9 offset:0\n"
    "s_waitcnt lgkmcnt(0)\n"
    "v_cmp_gt_u32 vcc, 2, v3\n"
    "s_cbranch_vccz .Ls4_cb76\n"
    "s_sub_u32 s100, s100, 1\n"
    "s_cselect_b32 s100, 0, s100\n"
    "s_cbranch_scc0 .Ls4_cs75\n"
    "s_branch .Ls4_cb76\n"
    ".Ls4_cs78:\n"
    "ds_read_b32 v3, v9 offset:0\n"
    "s_waitcnt lgkmcnt(0)\n"
    "v_cmp_gt_u32 vcc, 2, v3\n"
    "s_cbranch_vccz .Ls4_cb79\n"
    "s_sub_u32 s100, s100, 1\n"
    "s_cselect_b32 s100, 0, s100\n"
    "s_cbranch_scc0 .Ls4_cs78\n"
    "s_branch .Ls4_cb79\n"
    ".Ls4_cs80:\n"
    "ds_read_b32 v3, v9 offset:0\n"
    "s_waitcnt lgkmcnt(0)\n"
    "v_cmp_gt_u32 vcc, 3, v3\n"
    "s_cbranch_vccz .Ls4_cb81\n"
    "s_sub_u32 s100, s100, 1\n"
    "s_cselect_b32 s100, 0, s100\n"
    "s_cbranch_scc0 .Ls4_cs80\n"
    "s_branch .Ls4_cb81\n"
    ".Ls4_bl84:\n"
    "ds_read_b32 v15, v1\n"
    "s_waitcnt lgkmcnt(0)\n"
    "v_cmp_gt_u32 vcc, s72, v15\n"
    "s_cbranch_vccnz .Ls4_bl86\n"
    "s_swappc_b64 s[70:71], s[68:69]\n"
    "s_branch .Ls4_bl83\n"
    ".Ls4_bl86:\n"
    "s_sleep 1\n"
    "s_sub_u32 s100, s100, 1\n"
    "s_cselect_b32 s100, 0, s100\n"
    "s_cbranch_scc0 .Ls4_bl84\n"
    "s_branch .Ls4_bl85\n"
    ".Ls4_cs87:\n"
    "ds_read_b32 v3, v1 offset:1024\n"
    "s_waitcnt lgkmcnt(0)\n"
    "v_cmp_gt_u32 vcc, 4, v3\n"
    "s_cbranch_vccz .Ls4_cb88\n"
    "s_sub_u32 s100, s100, 1\n"
    "s_cselect_b32 s100, 0, s100\n"
    "s_cbranch_scc0 .Ls4_cs87\n"
    "s_branch .Ls4_cb88\n"
    ".Ls4_cs89:\n"
    "ds_read_b32 v3, v9 offset:0\n"
    "s_waitcnt lgkmcnt(0)\n"
    "v_cmp_gt_u32 vcc, 5, v3\n"
    "s_cbranch_vccz .Ls4_cb90\n"
    "s_sub_u32 s100, s100, 1\n"
    "s_cselect_b32 s100, 0, s100\n"
    "s_cbranch_scc0 .Ls4_cs89\n"
    "s_branch .Ls4_cb90\n"
    ".Ls4_cs93:\n"
    "ds_read_b32 v3, v9 offset:0\n"
    "s_waitcnt lgkmcnt(0)\n"
    "v_cmp_gt_u32 vcc, 6, v3\n"
    "s_cbranch_vccz .Ls4_cb94\n"
    "s_sub_u32 s100, s100, 1\n"
    "s_cselect_b32 s100, 0, s100\n"
    "s_cbranch_scc0 .Ls4_cs93\n"
    "s_branch .Ls4_cb94\n"
    ".Ls4_cs95:\n"
    "ds_read_b32 v3, v9 offset:0\n"
    "s_waitcnt lgkmcnt(0)\n"
    "v_cmp_gt_u32 vcc, 6, v3\n"
    "s_cbranch_vccz .Ls4_cb96\n"
    "s_sub_u32 s100, s100, 1\n"
    "s_cselect_b32 s100, 0, s100\n"
    "s_cbranch_scc0 .Ls4_cs95\n"
    "s_branch .Ls4_cb96\n"
    ".Ls4_cs97:\n"
    "ds_read_b32 v3, v1 offset:1024\n"
    "s_waitcnt lgkmcnt(0)\n"
    "v_cmp_gt_u32 vcc, 7, v3\n"
    "s_cbranch_vccz .Ls4_cb98\n"
    "s_sub_u32 s100, s100, 1\n"
    "s_cselect_b32 s100, 0, s100\n"
    "s_cbranch_scc0 .Ls4_cs97\n"
    "s_branch .Ls4_cb98\n"
    ".Ls4_cs99:\n"
    "ds_read_b32 v3, v1 offset:0\n"
    "s_waitcnt lgkmcnt(0)\n"
    "v_cmp_gt_u32 vcc, 16, v3\n"
    "s_cbranch_vccz .Ls4_cb100\n"
    "s_sub_u32 s100, s100, 1\n"
    "s_cselect_b32 s100, 0, s100\n"
    "s_cbranch_scc0 .Ls4_cs99\n"
    "s_branch .Ls4_cb100\n"
    ".Ls4_ps101:\n"
    "buffer_load_dwordx2 v[6:7], v25, s[24:27], s64 offen sc1\n"
    "s_waitcnt vmcnt(0)\n"
    "v_cmp_ne_u64 vcc, s[96:97], v[6:7]\n"
    "s_cbranch_vccz .Ls4_pb102\n"
    "s_sleep 2\n"
    "s_sub_u32 s100, s100, 1\n"
    "s_cselect_b32 s100, 0, s100\n"
    "s_cbranch_scc0 .Ls4_ps101\n"
    "s_branch .Ls4_pb102\n"
    ".Ls4_ns103:\n"
    "ds_read_b32 v3, v9 offset:64\n"
    "s_waitcnt lgkmcnt(0)\n"
    "v_cmp_gt_u32 vcc, 4, v3\n"
    "s_cbranch_vccz .Ls4_no105\n"
    "s_sleep 1\n"
    "s_sub_u32 s100, s100, 1\n"
    "s_cselect_b32 s100, 0, s100\n"
    "s_cbranch_scc0 .Ls4_ns103\n"
    "s_branch .Ls4_nb104\n"
    ".Ls4_no105:\n"
    "s_sleep 6\n"
    "s_branch .Ls4_nb104\n"
    ".Ls4_ps106:\n"
    "buffer_load_dwordx2 v[6:7], v10, s[24:27], s85 offen sc1\n"
    "s_waitcnt vmcnt(0)\n"
    "v_cmp_ne_u64 vcc, s[96:97], v[6:7]\n"
    "s_cbranch_vccz .Ls4_pb107\n"
    "s_sleep 2\n"
    "s_sub_u32 s100, s100, 1\n"
    "s_cselect_b32 s100, 0, s100\n"
    "s_cbranch_scc0 .Ls4_ps106\n"
    "s_branch .Ls4_pb107\n"
    ".Ls4_ns110:\n"
    "ds_read_b32 v3, v9 offset:96\n"
    "s_waitcnt lgkmcnt(0)\n"
    "v_cmp_gt_u32 vcc, 4, v3\n"
    "s_cbranch_vccz .Ls4_no112\n"
    "s_sleep 1\n"
    "s_sub_u32 s100, s100, 1\n"
    "s_cselect_b32 s100, 0, s100\n"
    "s_cbranch_scc0 .Ls4_ns110\n"
    "s_branch .Ls4_nb111\n"
    ".Ls4_no112:\n"
    "s_sleep 6\n"
    "s_branch .Ls4_nb111\n"
    ".Ls4_ps113:\n"
    "buffer_load_dwordx2 v[6:7], v10, s[24:27], s85 offen sc1\n"
    "s_waitcnt vmcnt(0)\n"
    "v_cmp_ne_u64 vcc, s[96:97], v[6:7]\n"
    "s_cbranch_vccz .Ls4_pb114\n"
    "s_sleep 2\n"
    "s_sub_u32 s100, s100, 1\n"
    "s_cselect_b32 s100, 0, s100\n"
    "s_cbranch_scc0 .Ls4_ps113\n"
    "s_branch .Ls4_pb114\n"
    ".Ls4_ns117:\n"
    "ds_read_b32 v3, v9 offset:128\n"
    "s_waitcnt lgkmcnt(0)\n"
    "v_cmp_gt_u32 vcc, 4, v3\n"
    "s_cbranch_vccz .Ls4_no119\n"
    "s_sleep 1\n"
    "s_sub_u32 s100, s100, 1\n"
    "s_cselect_b32 s100, 0, s100\n"
    "s_cbranch_scc0 .Ls4_ns117\n"
    "s_branch .Ls4_nb118\n"
    ".Ls4_no119:\n"
    "s_sleep 6\n"
    "s_branch .Ls4_nb118\n"
    ".Ls4_ps120:\n"
    "buffer_load_dwordx2 v[6:7], v10, s[24:27], s85 offen sc1\n"
    "s_waitcnt vmcnt(0)\n"
    "v_cmp_ne_u64 vcc, s[96:97], v[6:7]\n"
    "s_cbranch_vccz .Ls4_pb121\n"
    "s_sleep 2\n"
    "s_sub_u32 s100, s100, 1\n"
    "s_cselect_b32 s100, 0, s100\n"
    "s_cbranch_scc0 .Ls4_ps120\n"
    "s_branch .Ls4_pb121\n"
    ".Ls4_ns124:\n"
    "ds_read_b32 v3, v9 offset:160\n"
    "s_waitcnt lgkmcnt(0)\n"
    "v_cmp_gt_u32 vcc, 4, v3\n"
    "s_cbranch_vccz .Ls4_no126\n"
    "s_sleep 1\n"
    "s_sub_u32 s100, s100, 1\n"
    "s_cselect_b32 s100, 0, s100\n"
    "s_cbranch_scc0 .Ls4_ns124\n"
    "s_branch .Ls4_nb125\n"
    ".Ls4_no126:\n"
    "s_sleep 6\n"
    "s_branch .Ls4_nb125\n"
    ".Ls4_ps127:\n"
    "buffer_load_dwordx2 v[6:7], v10, s[24:27], s85 offen sc1\n"
    "s_waitcnt vmcnt(0)\n"
    "v_cmp_ne_u64 vcc, s[96:97], v[6:7]\n"
    "s_cbranch_vccz .Ls4_pb128\n"
    "s_sleep 2\n"
    "s_sub_u32 s100, s100, 1\n"
    "s_cselect_b32 s100, 0, s100\n"
    "s_cbranch_scc0 .Ls4_ps127\n"
    "s_branch .Ls4_pb128\n"
    ".Ls4_end:\n"
    :
    : "s"(__builtin_amdgcn_kernarg_segment_ptr()), "s"(blockIdx.x), "v"(threadIdx.x)
    : "v1", "v2", "v3", "v4", "v5", "v6", "v7", "v8", "v9", "v10", "v11", "v12", "v13", "v14", "v15", "v16", "v17", "v18", "v19", "v20", "v21", "v22", "v23", "v24", "v25", "v26", "v27", "v28", "v29", "v30", "v31", "v32", "v33", "v34", "v35", "v36", "v37", "v38", "v39", "v40", "v41", "v42", "v43", "v44", "v45", "v46", "v47", "v48", "v49", "v50", "v51", "v52", "v53", "v54", "v55", "v56", "v57", "v58", "v59", "v60", "v61", "v62", "v63", "v64", "v65", "v66", "v67", "v68", "v69", "v70", "v71", "v72", "v73", "v74", "v75", "v76", "v77", "v78", "v79", "v80", "v81", "v82", "v83", "v84", "v85", "v86", "v87", "v88", "v89", "v90", "v91", "v92", "v93", "v94", "v95", "v96", "v97", "v98", "v99", "v100", "v101", "v102", "v103", "v104", "v105", "v106", "v107", "v108", "v109", "v110", "v111", "v112", "v113", "v114", "v115", "v116", "v117", "v118", "v119", "v120", "v121", "v122", "v123", "v124", "v125", "v126", "v127", "v128", "v129", "v130", "v131", "v132", "v133", "v134", "v135", "v136", "v137", "v138", "v139", "v140", "v141", "v142", "v143", "v144", "v145", "v146", "v147", "v148", "v149", "v150", "v151", "v152", "v153", "v154", "v155", "v156", "v157", "v158", "v159", "v160", "v161", "v162", "v163", "v164", "v165", "v166", "v167", "v168", "v169", "v170", "v171", "v172", "v173", "v174", "v175", "v176", "v177", "v178", "v179", "v180", "v181", "v182", "v183", "v184", "v185", "v186", "v187", "v188", "v189", "v190", "v191", "v192", "v193", "v194", "v195", "v196", "v197", "v198", "v199", "v200", "v201", "v202", "v203", "v204", "v205", "v206", "v207", "v208", "v209", "v210", "v211", "v212", "v213", "v214", "v215", "v216", "v217", "v218", "v219", "v220", "v221", "v222", "v223", "v224", "v225", "v226", "v227", "v228", "v229", "v230", "v231", "v232", "v233", "v234", "v235", "v236", "v237", "v238", "v239", "v240", "v241", "v242", "v243", "v244", "v245", "v246", "v247", "v248", "v249", "v250", "v251", "v252", "v253", "v254", "v255",
      "s12", "s13", "s14", "s15", "s16", "s17", "s18", "s19", "s20", "s21", "s22", "s23", "s24", "s25", "s26", "s27", "s28", "s29", "s30", "s31", "s32", "s33", "s34", "s35", "s36", "s37", "s38", "s39", "s40", "s41", "s42", "s43", "s44", "s45", "s46", "s47", "s48", "s49", "s50", "s51", "s52", "s53", "s54", "s55", "s56", "s57", "s58", "s59", "s60", "s61", "s62", "s63", "s64", "s65", "s66", "s67", "s68", "s69", "s70", "s71", "s72", "s73", "s74", "s75", "s76", "s77", "s78", "s79", "s80", "s81", "s82", "s83", "s84", "s85", "s86", "s87", "s88", "s89", "s90", "s91", "s92", "s93", "s94", "s95", "s96", "s97", "s98", "s99", "s100", "s101", "vcc", "memory");
}

extern "C" void kernel_launch(void* const* d_in, const int* in_sizes, int n_in,
                              void* d_out, int out_size, void* d_ws, size_t ws_size,
                              hipStream_t stream) {
  Args a;
  const int order[15] = {0, 10, -2, 11, 12, 13, -1, 8, 1, 2, 3, 4, 5, 6, 7};
  for (int k = 0; k < 15; k++) {
    const int i = order[k];
    a.s[k].p = i >= 0 ? d_in[i] : (i == -1 ? d_out : d_ws);
    a.s[k].bytes = i >= 0 ? (uint32_t)in_sizes[i] * 2u : (i == -1 ? (uint32_t)out_size * 2u : 15728640u);
    a.s[k].fmt = 0x20000u;
  }
  (void)hipFuncSetAttribute((const void*)s4mix_kernel, hipFuncAttributeMaxDynamicSharedMemorySize, 163840);
  s4mix_kernel<<<256, 512, 163840, stream>>>(a);
}
```
